```python
import torch

def _softmax(x, axis=-1):
    return torch.softmax(x, dim=axis)
def _gelu(x):
    return torch.nn.functional.gelu(x, approximate="tanh")
def _silu(x):
    return torch.nn.functional.silu(x)
def _sigmoid(x):
    return torch.sigmoid(x)
def _layernorm(x, axis=-1, eps=1e-5):
    m = x.mean(dim=axis, keepdim=True)
    v = x.var(dim=axis, keepdim=True, unbiased=False)
    return (x - m) / torch.sqrt(v + eps)
def _scatter_add(tgt, idx, src, axis=0):
    return tgt.clone().index_add_(axis, idx, src)
def _segment_sum(src, seg, n):
    out = torch.zeros((n,) + src.shape[1:], dtype=src.dtype)
    return out.index_add_(0, seg, src)
def _pad(x, pw):
    flat = tuple(v for pair in reversed(pw) for v in pair)
    return torch.nn.functional.pad(x, flat)

kernel_name = 'interleave_layers__ssm_conv_h3__mixer_channel_mlp__prompt'

def setup_inputs():

    _w = (lambda a: (lambda t: t.to(torch.bfloat16) if t.is_floating_point() else t)(torch.as_tensor(a)))
    def _A_setup():
        B, S, D = 1, 2048, 512
        ks = 4
        return {
            "q": _w((lambda *s: torch.randn(*s, dtype=torch.bfloat16) * 0.05)(B, S, D)),
            "k": _w((lambda *s: torch.randn(*s, dtype=torch.bfloat16) * 0.4)(B, S, D)),
            "v": _w((lambda *s: torch.randn(*s, dtype=torch.bfloat16) * 0.4)(B, S, D)),
            "shift_k": _w((lambda *s: torch.randn(*s, dtype=torch.bfloat16) * 0.4)(D, ks) * 0.5),
            "ssm_k": _w((lambda *s: torch.randn(*s, dtype=torch.bfloat16) * 0.4)(D, S) * (1.0 / S ** 0.5)),
            "ssm_D": _w((lambda *s: torch.randn(*s, dtype=torch.bfloat16) * 0.4)(D)),
        }
    _A_in = _A_setup()

    def _B_setup():
        B, S, D, F = 1, 2048, 512, 1792
        return {
            "x": _w((lambda *s: torch.randn(*s, dtype=torch.bfloat16) * 0.05)(B, S, D)),
            "w1": _w((lambda *s: torch.randn(*s, dtype=torch.bfloat16) * 0.8)(D, F) * (D ** -0.5)),
            "b1": _w((lambda *s: torch.randn(*s, dtype=torch.bfloat16) * 0.8)(F)),
            "w2": _w((lambda *s: torch.randn(*s, dtype=torch.bfloat16) * 0.8)(F, D) * (F ** -0.5)),
            "b2": _w((lambda *s: torch.randn(*s, dtype=torch.bfloat16) * 0.2)(D)),
        }
    _B_in = _B_setup()

    return {
        "A_q": _A_in["q"],
        "A_k": _A_in["k"],
        "A_v": _A_in["v"],
        "A_shift_k": _A_in["shift_k"],
        "A_ssm_k": _A_in["ssm_k"],
        "A_ssm_D": _A_in["ssm_D"],
        "B_x": _B_in["x"],
        "B_w1": _B_in["w1"],
        "B_b1": _B_in["b1"],
        "B_w2": _B_in["w2"],
        "B_b2": _B_in["b2"],
    }

def reference(A_q, A_k, A_v, A_shift_k, A_ssm_k, A_ssm_D, B_x, B_w1, B_b1, B_w2, B_b2):
    def _A_ref(q, k, v, shift_k, ssm_k, ssm_D):
        B, S, D = q.shape
        ks = shift_k.shape[-1]
        k_pad = _pad(k, ((0, 0), (ks - 1, 0), (0, 0)))
        k_shift = torch.zeros_like(k)
        for t in range(ks):
            k_shift = k_shift + k_pad[:, t:t + S, :] * shift_k[:, ks - 1 - t][None, None, :]
        kv = k_shift * v
        L = 2 * S
        kv_pad = _pad(kv, ((0, 0), (0, S), (0, 0)))
        kf_pad = _pad(ssm_k, ((0, 0), (0, S)))
        kv_f = (lambda x, axes=(-2,-1): torch.fft.rfft2(x.float(), dim=axes))(kv_pad[:, None, :, :], axes=(1, 2))
        kf_f = (lambda x, axes=(-2,-1): torch.fft.rfft2(x.float(), dim=axes))(kf_pad[:, None, :], axes=(1, 2))
        kf_f = (lambda x, *d: x.permute(*d))(kf_f, 1, 2, 0)
        y_f = kv_f * kf_f[None, :, :, :]
        y = (lambda x, s, axes=(-2,-1): torch.fft.irfft2(x, s=s, dim=axes).to(torch.bfloat16))(y_f, (1, L), axes=(1, 2))[:, 0, :S, :]
        y = y + kv * ssm_D[None, None, :]
        return q * y

    def _B_ref(x, w1, b1, w2, b2):
        y = _layernorm(x, axis=-1)
        h = _gelu(torch.einsum("bsd,df->bsf", y, w1) + b1)
        return torch.einsum("bsf,fd->bsd", h, w2) + b2

    x = A_q
    x = x + _A_ref(_layernorm(x, axis=-1), A_k, A_v, A_shift_k, A_ssm_k, A_ssm_D)
    x = x + _B_ref(_layernorm(x, axis=-1), B_w1, B_b1, B_w2, B_b2)
    x = x + _A_ref(_layernorm(x, axis=-1), A_k, A_v, A_shift_k, A_ssm_k, A_ssm_D)
    x = x + _B_ref(_layernorm(x, axis=-1), B_w1, B_b1, B_w2, B_b2)
    return x
```

```cpp
#include <hip/hip_runtime.h>
#include <hip/hip_bf16.h>
#include <stdint.h>

#define GAS __attribute__((address_space(1)))
#define LAS __attribute__((address_space(3)))
typedef unsigned v4u __attribute__((ext_vector_type(4)));
typedef unsigned v2u __attribute__((ext_vector_type(2)));
typedef float f32x2 __attribute__((ext_vector_type(2)));
typedef float f32x4 __attribute__((ext_vector_type(4)));
typedef float f32x16 __attribute__((ext_vector_type(16)));
typedef __bf16 bf16x8 __attribute__((ext_vector_type(8)));
typedef __bf16 bf16x2 __attribute__((ext_vector_type(2)));
typedef short s16x4 __attribute__((ext_vector_type(4)));
typedef GAS unsigned long long gu64;
typedef __amdgpu_buffer_rsrc_t rsrc_t;
#define RLX_AGENT __ATOMIC_RELAXED, __HIP_MEMORY_SCOPE_AGENT
#define SC1 16
#define SCHED() __builtin_amdgcn_sched_barrier(0)

namespace {
constexpr int S_ = 2048, D_ = 512, F_ = 1792;
constexpr int WS_GEN = 0;
constexpr int WS_FLAG = 4096;
constexpr int FLAG_ARR = 256 * 128;
constexpr int WS_SG = WS_FLAG + 6 * FLAG_ARR;
constexpr int WS_ST1 = WS_SG + 32 * 4 * 8 * 256;
constexpr int WS_ST3 = WS_ST1 + 128 * 8 * 128;
constexpr int WS_U = 1 << 20;
constexpr int WS_YT = WS_U + (4 << 20);
constexpr int WS_X1 = WS_YT + (2 << 20);
constexpr int WS_X3 = WS_X1 + (2 << 20);
constexpr int WS_H1 = WS_X3 + (2 << 20);
constexpr int WS_H2 = WS_H1 + 128 * 56 * 1024;
constexpr int WS_STAMP = WS_H2 + 128 * 56 * 1024;
constexpr int WS_END = WS_STAMP + (1 << 20);
static_assert(WS_ST3 + 128 * 8 * 128 <= WS_U, "ws map");
constexpr int LDS_BYTES = 163840;
constexpr int L_W1 = LDS_BYTES - 114688;
constexpr int L_TAB = 32768;
constexpr int L_RED = 0;
constexpr int L_YT = 0;
constexpr int L_H = 0;
constexpr int H_CS = 4416, H_CH = 4 * H_CS;
constexpr int L_U = 35840;
constexpr int U_CS = 6144;
constexpr int L_CX = 60416;
constexpr float EPS = 1e-5f;
constexpr unsigned SPIN_CAP = 1u << 16;

struct Params {
  const unsigned short *q, *k, *v, *sk, *hk, *hD, *w1, *b1, *w2, *b2;
  unsigned short* out;
  unsigned char* ws;
};

__device__ __forceinline__ float u2f(unsigned x) { return __builtin_bit_cast(float, x); }
__device__ __forceinline__ unsigned f2u(float x) { return __builtin_bit_cast(unsigned, x); }
__device__ __forceinline__ float blo(unsigned x) { return __builtin_bit_cast(float, x << 16); }
__device__ __forceinline__ float bhi(unsigned x) { return __builtin_bit_cast(float, x & 0xffff0000u); }
__device__ __forceinline__ unsigned pk(float a, float b) { bf16x2 r = {(__bf16)a, (__bf16)b}; return __builtin_bit_cast(unsigned, r); }
#define MF16(acc, a, b) asm volatile("v_mfma_f32_16x16x32_bf16 %0, %1, %2, %0" : "+v"(acc) : "v"(a), "v"(b))
#define MF16A(acc, a, b) asm volatile("v_mfma_f32_16x16x32_bf16 %0, %1, %2, %0" : "+v"(acc) : "a"(a), "v"(b))
#define MF16A0(acc, a, b) asm volatile("v_mfma_f32_16x16x32_bf16 %0, %1, %2, 0" : "=&v"(acc) : "a"(a), "v"(b))
#define MF16Z(acc, a, b) asm volatile("v_mfma_f32_16x16x32_bf16 %0, %1, %2, 0" : "=&v"(acc) : "v"(a), "v"(b))
#define MF32(acc, a, b) asm volatile("v_mfma_f32_32x32x16_bf16 %0, %1, %2, %0" : "+v"(acc) : "v"(a), "v"(b))
#define MF32Z(acc, a, b) asm volatile("v_mfma_f32_32x32x16_bf16 %0, %1, %2, 0" : "=&v"(acc) : "v"(a), "v"(b))
#define NOPS(n) do { SCHED(); asm volatile("s_nop " #n); SCHED(); } while (0)
__device__ __forceinline__ rsrc_t mk(const void* p, int bytes) { return __builtin_amdgcn_make_buffer_rsrc((void*)p, 0, bytes, 0x00020000); }
__device__ __forceinline__ v4u ld128(rsrc_t r, int voff, int soff) { return __builtin_amdgcn_raw_buffer_load_b128(r, voff, soff, SC1); }
__device__ __forceinline__ void st128(v4u v, rsrc_t r, int voff, int soff) { __builtin_amdgcn_raw_buffer_store_b128(v, r, voff, soff, SC1); }
__device__ __forceinline__ float rowsum4(float x) {
  float a = x, c = x;
  asm("s_nop 1\n\tv_permlane16_swap_b32 %0, %1" : "+v"(a), "+v"(c));
  a += c; c = a;
  asm("s_nop 1\n\tv_permlane32_swap_b32 %0, %1" : "+v"(a), "+v"(c));
  return a + c;
}
#define DRAIN() asm volatile("s_waitcnt vmcnt(0)" ::: "memory")
#define BAR() do { asm volatile("s_waitcnt lgkmcnt(0)" ::: "memory"); __builtin_amdgcn_s_barrier(); asm volatile("" ::: "memory"); } while (0)
#define STAMP(i)

__device__ __forceinline__ void dma16(rsrc_t r, int voff, unsigned lds_dst) {
  unsigned keep;
  asm volatile("s_mov_b32 %0, m0\n\ts_mov_b32 m0, %3\n\ts_nop 0\n\tbuffer_load_dwordx4 %1, %2, 0 offen lds\n\ts_mov_b32 m0, %0"
               : "=&s"(keep) : "v"(voff), "s"(r), "s"(lds_dst) : "memory");
}
__device__ __forceinline__ void publish(gu64* flag, unsigned long long tag, int tid) {
  DRAIN();
  BAR();
  if (tid == 0) __hip_atomic_store(flag, tag, RLX_AGENT);
}
__device__ __forceinline__ void await(gu64* base, int stride, int n, unsigned long long tag, int w, int lane) {
  if (w == 0) {
    gu64* f = base + (lane < n ? lane : 0) * stride * 16;
#pragma unroll 1
    for (unsigned spins = 0; spins < SPIN_CAP; ++spins) {
      unsigned long long x = __hip_atomic_load(f, RLX_AGENT);
      if (__all(x == tag)) break;
      __builtin_amdgcn_s_sleep(1);
    }
  }
  __builtin_amdgcn_fence(__ATOMIC_ACQUIRE, "wavefront");
  BAR();
}
}

__global__ void __launch_bounds__(256, 1) h3_mixer_kernel(Params p) {
  extern __shared__ __attribute__((aligned(16))) unsigned char smem[];
  LAS unsigned char* L = (LAS unsigned char*)smem;
  const unsigned Lb = (unsigned)(uintptr_t)L;
  const int tid = threadIdx.x, lane = tid & 63, w = __builtin_amdgcn_readfirstlane(tid >> 6);
  const int b = blockIdx.x;
  const int P = (b & 7) + 8 * (b >> 7), m = (b >> 3) & 15;
  const int th = m >> 3, cb = m & 7, tb = 2 * P + th;
  const int s = lane & 15, q4 = lane >> 4, l16 = lane * 16;
  STAMP(0);
  const rsrc_t rw = mk(p.ws, WS_END);
  gu64* flags = (gu64*)(p.ws + WS_FLAG);
  const unsigned long long gen = __hip_atomic_load((gu64*)(p.ws + WS_GEN), RLX_AGENT);

  {
    const rsrc_t rk = mk(p.k, S_ * D_ * 2), rv = mk(p.v, S_ * D_ * 2);
    const int g = (lane & 3) + 4 * (w & 1), cpl = (lane >> 2) + 16 * (w >> 1);
    const int t0 = 64 * tb + 8 * g, ch = 64 * cb + 2 * cpl;
    unsigned kr[12], vr[9];
#pragma unroll
    for (int i = 0; i < 12; ++i) kr[i] = __builtin_amdgcn_raw_buffer_load_b32(rk, (t0 - 4 + i) * 1024 + ch * 2, 0, 0);
#pragma unroll
    for (int i = 0; i < 9; ++i) vr[i] = __builtin_amdgcn_raw_buffer_load_b32(rv, (t0 - 1 + i) * 1024 + ch * 2, 0, 0);
    const v4u sk = *(const GAS v4u*)(p.sk + ch * 4);
    const f32x2 s0 = {blo(sk.x), blo(sk.z)}, s1 = {bhi(sk.x), bhi(sk.z)}, s2 = {blo(sk.y), blo(sk.w)}, s3 = {bhi(sk.y), bhi(sk.w)};
    f32x2 kf[12], uu[9];
#pragma unroll
    for (int i = 0; i < 12; ++i) kf[i] = f32x2{blo(kr[i]), bhi(kr[i])};
#pragma unroll
    for (int i = 0; i < 9; ++i) {
      f32x2 a = kf[i] * s3;
      a = kf[i + 1] * s2 + a;
      a = kf[i + 2] * s1 + a;
      a = kf[i + 3] * s0 + a;
      uu[i] = a * f32x2{blo(vr[i]), bhi(vr[i])};
    }
    const int y = 31 - tb;
    const int base = WS_U + ch * 8192 + y * 128 + 16 * ((7 - g) ^ ((y >> 1) & 7));
#pragma unroll
    for (int c = 0; c < 2; ++c) {
      v4u o0 = {pk(uu[7][c], uu[6][c]), pk(uu[5][c], uu[4][c]), pk(uu[3][c], uu[2][c]), pk(uu[1][c], uu[0][c])};
      v4u o1 = {pk(uu[8][c], uu[7][c]), pk(uu[6][c], uu[5][c]), pk(uu[4][c], uu[3][c]), pk(uu[2][c], uu[1][c])};
      __builtin_amdgcn_raw_buffer_store_b128(o0, rw, base + c * 8192, 0, SC1);
      __builtin_amdgcn_raw_buffer_store_b128(o1, rw, base + c * 8192 + 4096, 0, SC1);
    }
  }
  const unsigned long long tag = __builtin_amdgcn_readfirstlane((unsigned)gen) + 1ull + ((unsigned long long)__builtin_amdgcn_readfirstlane((unsigned)(gen >> 32)) << 32);
  const unsigned tag32 = (unsigned)tag;
  {
    v4u z = {0u, ~tag32, 0u, ~tag32};
    if (q4 == 0) __builtin_amdgcn_raw_buffer_store_b128(z, rw, WS_SG + ((tb * 4 + w) * 8 + cb) * 256 + s * 16, 0, SC1);
  }
  STAMP(1);
  publish(flags + (0 * 256 + tb * 8 + cb) * 16, tag, tid);
  STAMP(2);

  const int cp = b;
  {
    const rsrc_t rh = mk(p.hk, D_ * S_ * 2);
#pragma unroll
    for (int i = 0; i < 9; ++i) {
      const int ins = w * 9 + i;
      if (ins < 35) {
        const int Lc = ins * 64 + lane;
        const int cc = Lc / 276, mm = Lc - cc * 276, c = cc & 3;
        int off = (2 * cp + (cc >> 2)) * 4096 + 4 * c - 124 + 16 * mm;
        if (mm < 7 || mm >= 263 || cc >= 8 || (mm == 7 && c < 3)) off = 0x7ffffff0;
        dma16(rh, off, Lb + L_H + ins * 1024);
      }
    }
    const v4u z = {0u, 0u, 0u, 0u};
#pragma unroll
    for (int i = 0; i < 2; ++i) *(LAS v4u*)(L + L_U + w * U_CS + 4096 + i * 1024 + lane * 16) = z;
  }
  const int tokl = 32 * (w >> 1) + 16 * ((s & 7) >> 2) + 8 * (w & 1) + 2 * (s & 3) + (s >> 3);
  const int tok = 64 * tb + tokl;
  float muq, rq;
  v4u xr[2];
  {
    const GAS v4u* qrow = (const GAS v4u*)(p.q + tok * D_);
    v4u qf[16];
#pragma unroll
    for (int i = 0; i < 16; ++i) qf[i] = qrow[4 * i + q4];
    xr[0] = qrow[8 * cb + 2 * q4];
    xr[1] = qrow[8 * cb + 2 * q4 + 1];
    v4u ones = {0x3f803f80u, 0x3f803f80u, 0x3f803f80u, 0x3f803f80u};
    asm volatile("" : "+v"(ones));
    NOPS(1);
    f32x4 a1, a2;
    MF16Z(a1, ones, qf[0]); MF16Z(a2, qf[0], qf[0]);
#pragma unroll
    for (int i = 1; i < 16; ++i) { MF16(a1, ones, qf[i]); MF16(a2, qf[i], qf[i]); }
    NOPS(7);
    const float dsel = (s & 2) ? ((s & 1) ? a2[3] : a2[2]) : ((s & 1) ? a2[1] : a2[0]);
    const float sq = rowsum4((s >> 2) == q4 ? dsel : 0.f);
    muq = a1[0] * (1.f / D_);
    rq = __builtin_amdgcn_rsqf(fmaxf(sq * (1.f / D_) - muq * muq, 0.f) + EPS);
  }
  DRAIN();
  BAR();
  if (tid < 6) {
    const int c2 = tid / 3, c = tid - 3 * c2;
    const v4u h0 = *(const GAS v4u*)(p.hk + (2 * cp + c2) * S_);
    v4u o = {0u, c == 2 ? h0.x : 0u, c == 2 ? h0.y : (c == 1 ? h0.x : 0u), c == 2 ? h0.z : (c == 1 ? h0.y : h0.x)};
    *(LAS v4u*)(L + L_H + c2 * H_CH + c * H_CS + 7 * 16) = o;
  }
  STAMP(3);
  await(flags + (0 * 256 + (cp >> 5)) * 16, 8, 32, tag, w, lane);
  STAMP(4);
  {
    v4u ut[4];
#pragma unroll
    for (int i = 0; i < 4; ++i) ut[i] = ld128(rw, l16, WS_U + (2 * cp) * 8192 + w * 4096 + i * 1024);
#pragma unroll
    for (int i = 0; i < 4; ++i) *(LAS v4u*)(L + L_U + w * U_CS + i * 1024 + lane * 16) = ut[i];
  }
  BAR();
  STAMP(5);
  {
    const int chl = w >> 1, kh = w & 1;
    const int hh = lane >> 5, n = lane & 31, pp = n >> 4, a_ = n & 15;
    LAS unsigned char* Ab = L + L_H + chl * H_CH + (n & 3) * H_CS + 16 * ((n >> 2) + hh) + kh * 1024;
    const int Y0 = 31 - a_ + 8 * kh;
    const int ub = L_U + (chl * 2 + pp) * U_CS;
    f32x16 acc0, acc1;
    v4u fa[2][4];
    auto cload = [&](int kk, v4u (&f)[4]) {
      const int yy = Y0 + (kk >> 2);
      LAS unsigned char* bp = L + (ub + yy * 128 + ((32 * (kk & 3)) ^ ((hh ^ ((yy >> 1) & 7)) << 4)));
      f[1] = *(LAS v4u*)(bp - 2048);
      f[0] = *(LAS v4u*)(Ab + 32 * kk);
      f[2] = *(LAS v4u*)(bp);
      f[3] = *(LAS v4u*)(Ab + 32 * kk + 2048);
    };
    cload(0, fa[0]);
#pragma unroll
    for (int kk = 0; kk < 32; ++kk) {
      if (kk + 1 < 32) cload(kk + 1, fa[(kk + 1) & 1]);
      SCHED();
      v4u(&f)[4] = fa[kk & 1];
      if (kk == 0) { MF32Z(acc1, f[0], f[1]); MF32Z(acc0, f[0], f[2]); }
      else { MF32(acc1, f[0], f[1]); MF32(acc0, f[0], f[2]); }
      MF32(acc1, f[3], f[2]);
      SCHED();
    }
    NOPS(11);
    LAS f32x4* cx = (LAS f32x4*)(L + L_CX);
#pragma unroll
    for (int g = 0; g < 4; ++g) {
      f32x4 o;
#pragma unroll
      for (int i = 0; i < 4; ++i) o[i] = kh ? acc0[4 * g + i] : acc1[4 * g + i];
      cx[(w * 4 + g) * 64 + lane] = o;
    }
    BAR();
    const int a = a_ + 16 * kh, yr = 31 - a;
    const float dD = blo(p.hD[2 * cp + chl]);
    float yv[16];
#pragma unroll
    for (int g = 0; g < 4; ++g) {
      const f32x4 o = cx[((w ^ 1) * 4 + g) * 64 + lane];
      const v4u uc = *(LAS v4u*)(L + ub + yr * 128 + 16 * ((7 - 2 * g - hh) ^ ((yr >> 1) & 7)));
#pragma unroll
      for (int i = 0; i < 4; ++i) yv[4 * g + i] = (kh ? acc1[4 * g + i] : acc0[4 * g + i]) + o[i] + blo(uc[3 - i]) * dD;
    }
#pragma unroll
    for (int e = 0; e < 2; ++e) {
      v4u o = {pk(yv[8 * e], yv[8 * e + 1]), pk(yv[8 * e + 2], yv[8 * e + 3]), pk(yv[8 * e + 4], yv[8 * e + 5]), pk(yv[8 * e + 6], yv[8 * e + 7])};
      __builtin_amdgcn_raw_buffer_store_b128(o, rw, WS_YT + (a * 512 + 2 * cp + chl) * 128 + 64 * e + 16 * (2 * hh + pp), 0, SC1);
    }
  }
  STAMP(6);
  publish(flags + (1 * 256 + cp) * 16, tag, tid);
  STAMP(7);

  {
    const rsrc_t r1 = mk(p.w1, D_ * F_ * 2);
#pragma unroll
    for (int i = 0; i < 28; ++i) {
      const int ins = w * 28 + i;
      const int Lc = ins * 64 + lane;
      const int rho = Lc / 14, cc = Lc - rho * 14;
      const int d = 64 * (rho >> 6) + 16 * ((rho >> 2) & 3) + 8 * ((rho >> 5) & 1) + 4 * ((rho >> 4) & 1) + (rho & 3);
      dma16(r1, d * (F_ * 2) + 224 * m + 16 * cc, Lb + L_W1 + ins * 1024);
    }
  }
  v4u w2f[14][4];
  {
    const rsrc_t r2 = mk(p.w2, F_ * D_ * 2);
    const int co = (64 * cb + 4 * s) * 2, coP = co + 4 * q4 * (D_ * 2), coS = co + (112 * (q4 >> 1) + 8 * (q4 & 1)) * (D_ * 2);
#pragma unroll
    for (int kk = 0; kk < 14; ++kk) {
      const int pr = kk / 7, j = kk % 7;
      v2u rows[8];
#pragma unroll
      for (int i = 0; i < 8; ++i) {
        if (j == 3) rows[i] = __builtin_amdgcn_raw_buffer_load_b64(r2, coS, (112 * (4 * w + 2 * pr) + 96 + i) * (D_ * 2), 0);
        else rows[i] = __builtin_amdgcn_raw_buffer_load_b64(r2, coP, (112 * (4 * w + 2 * pr + (j > 3)) + 32 * (j > 3 ? j - 4 : j) + 16 * (i >> 2) + (i & 3)) * (D_ * 2), 0);
      }
#pragma unroll
      for (int jt = 0; jt < 4; ++jt)
#pragma unroll
        for (int ii = 0; ii < 4; ++ii)
          w2f[kk][jt][ii] = __builtin_amdgcn_perm(rows[2 * ii + 1][jt >> 1], rows[2 * ii][jt >> 1], (jt & 1) ? 0x07060302u : 0x05040100u);
#pragma unroll
      for (int jt = 0; jt < 4; ++jt) asm volatile("" : "+a"(w2f[kk][jt]));
    }
  }
  STAMP(8);
  await(flags + (1 * 256 + cb * 32) * 16, 1, 32, tag, w, lane);
  STAMP(9);
  unsigned yp[8];
  {
    v4u yt[2];
#pragma unroll
    for (int i = 0; i < 2; ++i) yt[i] = ld128(rw, l16, WS_YT + (tb * 512 + 64 * cb) * 128 + (w * 2 + i) * 1024);
#pragma unroll
    for (int i = 0; i < 2; ++i) *(LAS v4u*)(L + L_YT + (w * 2 + i) * 1024 + lane * 16) = yt[i];
    BAR();
#pragma unroll
    for (int s4 = 0; s4 < 4; ++s4) {
      const v2u r = __builtin_bit_cast(v2u, __builtin_amdgcn_ds_read_tr16_b64_v4i16((LAS s16x4*)(L + L_YT + (16 * q4 + 4 * s4 + (s >> 2)) * 128 + 32 * w + 8 * (s & 3))));
      yp[2 * s4] = r.x; yp[2 * s4 + 1] = r.y;
    }
  }
  auto gate_publish = [&](const float (&x)[16], float mu, float r, int xoff, int stoff) {
    float s1 = 0.f, s2 = 0.f;
#pragma unroll
    for (int i = 0; i < 8; ++i) {
      asm volatile("" : "+v"(yp[i]));
      const float a0 = x[2 * i] + (x[2 * i] - mu) * r * blo(yp[i]), a1 = x[2 * i + 1] + (x[2 * i + 1] - mu) * r * bhi(yp[i]);
      const unsigned u = pk(a0, a1);
      xr[i >> 2][i & 3] = u;
      const float c0 = blo(u), c1 = bhi(u);
      s1 += c0 + c1;
      s2 = fmaf(c0, c0, fmaf(c1, c1, s2));
    }
#pragma unroll
    for (int e = 0; e < 2; ++e) st128(xr[e], rw, l16, xoff + ((tb * 4 + w) * 16 + 2 * cb + e) * 1024);
    s1 = rowsum4(s1); s2 = rowsum4(s2);
    v2u st = {f2u(s1), f2u(s2)};
    if (q4 == 0) __builtin_amdgcn_raw_buffer_store_b64(st, rw, s * 8, stoff + ((tb * 4 + w) * 8 + cb) * 128, SC1);
  };
  {
    float x[16];
#pragma unroll
    for (int i = 0; i < 8; ++i) { x[2 * i] = blo(xr[i >> 2][i & 3]); x[2 * i + 1] = bhi(xr[i >> 2][i & 3]); }
    gate_publish(x, muq, rq, WS_X1, WS_ST1);
  }
  STAMP(10);
  publish(flags + (2 * 256 + P * 16 + m) * 16, tag, tid);
  STAMP(11);
  LAS unsigned char* Wa = L + L_W1 + (lane >> 2) * 224 + 8 * (lane & 3);
  auto w1frag = [&](int ks, int ft) {
    const v2u lo = __builtin_bit_cast(v2u, __builtin_amdgcn_ds_read_tr16_b64_v4i16((LAS s16x4*)(Wa + ks * 7168 + 32 * ft)));
    const v2u hi = __builtin_bit_cast(v2u, __builtin_amdgcn_ds_read_tr16_b64_v4i16((LAS s16x4*)(Wa + ks * 7168 + 3584 + 32 * ft)));
    return v4u{lo.x, lo.y, hi.x, hi.y};
  };
  {
    v4u ones = {0x3f803f80u, 0x3f803f80u, 0x3f803f80u, 0x3f803f80u};
    asm volatile("" : "+v"(ones));
    NOPS(1);
#pragma unroll
    for (int fi = 0; fi < 2; ++fi) {
      const int ft = 2 * w + fi;
      if (ft < 7) {
        f32x4 c;
        v4u a[16];
#pragma unroll
        for (int ks = 0; ks < 16; ++ks) a[ks] = w1frag(ks, ft);
        MF16Z(c, a[0], ones);
#pragma unroll
        for (int ks = 1; ks < 16; ++ks) MF16(c, a[ks], ones);
        NOPS(7);
        if (s == 0) *(LAS f32x4*)(L + L_TAB + (16 * ft + 4 * q4) * 4) = c;
      }
    }
    if (tid < 112) *(LAS float*)(L + L_TAB + 512 + tid * 4) = blo(p.b1[112 * m + tid]);
  }
  if (tid < 64) *(LAS float*)(L + L_TAB + 1024 + tid * 4) = blo(p.b2[64 * cb + tid]);

#pragma unroll 1
  for (int layer = 0; layer < 2; ++layer) {
    const int xoff = layer ? WS_X3 : WS_X1, stoff = layer ? WS_ST3 : WS_ST1, hoff = layer ? WS_H2 : WS_H1;
    await(flags + ((layer ? 4 : 2) * 256 + P * 16) * 16, 1, 16, tag, w, lane);
    STAMP(12 + 8 * layer);
    {
      const int TT0 = 8 * P + 2 * w;
      v2u st[2][2];
#pragma unroll
      for (int tt = 0; tt < 2; ++tt)
#pragma unroll
        for (int c = 0; c < 2; ++c) st[tt][c] = __builtin_amdgcn_raw_buffer_load_b64(rw, q4 * 256 + s * 8 + c * 128, stoff + (TT0 + tt) * 1024, SC1);
      v4u xf[2][16];
#pragma unroll
      for (int ks = 0; ks < 16; ++ks)
#pragma unroll
        for (int tt = 0; tt < 2; ++tt) xf[tt][ks] = ld128(rw, l16, xoff + ((TT0 + tt) * 16 + ks) * 1024);
      SCHED();
      float mu[2], sg[2], cA[2], cB[2];
#pragma unroll
      for (int tt = 0; tt < 2; ++tt) {
        const float s1 = rowsum4(u2f(st[tt][0].x) + u2f(st[tt][1].x));
        const float s2 = rowsum4(u2f(st[tt][0].y) + u2f(st[tt][1].y));
        mu[tt] = s1 * (1.f / D_);
        const float var = fmaxf(s2 * (1.f / D_) - mu[tt] * mu[tt], 0.f);
        const float r1 = __builtin_amdgcn_rsqf(var + EPS);
        const float r = r1 * __builtin_amdgcn_rsqf(var * r1 * r1 + EPS);
        sg[tt] = __builtin_amdgcn_rcpf(r);
        cB[tt] = -2.302208198f * r;
        cA[tt] = cB[tt] * 0.044715f * r * r;
      }
      f32x4 acc[2][7];
#pragma unroll
      for (int ft = 0; ft < 7; ++ft) {
        const f32x4 c = *(LAS f32x4*)(L + L_TAB + (16 * ft + 4 * q4) * 4), bb = *(LAS f32x4*)(L + L_TAB + 512 + (16 * ft + 4 * q4) * 4);
#pragma unroll
        for (int tt = 0; tt < 2; ++tt) acc[tt][ft] = bb * sg[tt] - c * mu[tt];
      }
      constexpr int AW = 5;
      v4u af[AW];
#pragma unroll
      for (int n = 0; n < AW - 1; ++n) af[n] = w1frag(n / 7, n % 7);
      NOPS(1);
#pragma unroll
      for (int n = 0; n < 112; ++n) {
        if (n + AW - 1 < 112) af[(n + AW - 1) % AW] = w1frag((n + AW - 1) / 7, (n + AW - 1) % 7);
        SCHED();
        MF16(acc[0][n % 7], af[n % AW], xf[0][n / 7]);
        MF16(acc[1][n % 7], af[n % AW], xf[1][n / 7]);
        SCHED();
      }
      NOPS(7);
      const int k2b = 7 * (m >> 1);
#pragma unroll
      for (int tt = 0; tt < 2; ++tt) {
        unsigned hp[14];
#pragma unroll
        for (int ft = 0; ft < 7; ++ft) {
          float g[4];
#pragma unroll
          for (int r = 0; r < 4; ++r) {
            const float y = acc[tt][ft][r];
            const float e = __builtin_amdgcn_exp2f(y * fmaf(y * y, cA[tt], cB[tt]));
            g[r] = y * __builtin_amdgcn_rcpf(fmaf(e, sg[tt], sg[tt]));
          }
          hp[2 * ft] = pk(g[0], g[1]); hp[2 * ft + 1] = pk(g[2], g[3]);
        }
        const int hb = hoff + ((TT0 + tt) * 56 + k2b) * 1024;
#pragma unroll
        for (int pr = 0; pr < 3; ++pr) {
          v4u o = {hp[4 * pr], hp[4 * pr + 1], hp[4 * pr + 2], hp[4 * pr + 3]};
          st128(o, rw, l16, hb + ((m & 1) * 4 + pr) * 1024);
        }
        v2u o = {hp[12], hp[13]};
        __builtin_amdgcn_raw_buffer_store_b64(o, rw, (q4 >> 1) * 256 + s * 16 + (q4 & 1) * 8, hb + 3 * 1024 + (m & 1) * 512, SC1);
      }
    }
    STAMP(13 + 8 * layer);
    publish(flags + ((layer ? 5 : 3) * 256 + P * 16 + m) * 16, tag, tid);
    STAMP(14 + 8 * layer);
    await(flags + ((layer ? 5 : 3) * 256 + P * 16) * 16, 1, 16, tag, w, lane);
    STAMP(15 + 8 * layer);
    float xv[16];
    {
      constexpr int WIN = 6;
      v4u hf[WIN][4];
      const int hb = hoff + (tb * 4 * 56 + 14 * w) * 1024;
#pragma unroll
      for (int kk = 0; kk < WIN; ++kk)
#pragma unroll
        for (int t = 0; t < 4; ++t) hf[kk][t] = ld128(rw, l16, hb + (t * 56 + kk) * 1024);
      SCHED();
      f32x4 acc[4][4];
#pragma unroll
      for (int kk = 0; kk < 14; ++kk) {
#pragma unroll
        for (int t = 0; t < 4; ++t) {
#pragma unroll
          for (int jt = 0; jt < 4; ++jt) {
            if (kk == 0) MF16A0(acc[t][jt], w2f[kk][jt], hf[kk % WIN][t]);
            else MF16A(acc[t][jt], w2f[kk][jt], hf[kk % WIN][t]);
          }
          SCHED();
          if (kk + WIN < 14) hf[kk % WIN][t] = ld128(rw, l16, hb + (t * 56 + kk + WIN) * 1024);
          SCHED();
        }
      }
      NOPS(7);
      LAS f32x4* red = (LAS f32x4*)(L + L_RED);
      const int hi = w >> 1, lo = w & 1;
      f32x4 k0[4], k1[4];
#pragma unroll
      for (int jt = 0; jt < 4; ++jt) {
        red[((w ^ 2) * 8 + jt) * 64 + lane] = hi ? acc[0][jt] : acc[2][jt];
        red[((w ^ 2) * 8 + 4 + jt) * 64 + lane] = hi ? acc[1][jt] : acc[3][jt];
        k0[jt] = hi ? acc[2][jt] : acc[0][jt];
        k1[jt] = hi ? acc[3][jt] : acc[1][jt];
      }
      BAR();
#pragma unroll
      for (int jt = 0; jt < 4; ++jt) { k0[jt] += red[(w * 8 + jt) * 64 + lane]; k1[jt] += red[(w * 8 + 4 + jt) * 64 + lane]; }
      asm volatile("s_waitcnt lgkmcnt(0)" ::: "memory");
#pragma unroll
      for (int jt = 0; jt < 4; ++jt) red[(w * 8 + jt) * 64 + lane] = lo ? k0[jt] : k1[jt];
      BAR();
#pragma unroll
      for (int jt = 0; jt < 4; ++jt) {
        const f32x4 o = (lo ? k1[jt] : k0[jt]) + red[((w ^ 1) * 8 + jt) * 64 + lane];
#pragma unroll
        for (int r = 0; r < 4; ++r) xv[4 * r + jt] = o[r];
      }
#pragma unroll
      for (int i = 0; i < 4; ++i) {
        const f32x4 bb = *(LAS f32x4*)(L + L_TAB + 1024 + (16 * q4 + 4 * i) * 4);
        xv[4 * i] += bb[0] + blo(xr[i >> 1][2 * (i & 1)]); xv[4 * i + 1] += bb[1] + bhi(xr[i >> 1][2 * (i & 1)]);
        xv[4 * i + 2] += bb[2] + blo(xr[i >> 1][2 * (i & 1) + 1]); xv[4 * i + 3] += bb[3] + bhi(xr[i >> 1][2 * (i & 1) + 1]);
      }
    }
    STAMP(16 + 8 * layer);
    if (layer == 0) {
      float s1 = 0.f, s2 = 0.f;
#pragma unroll
      for (int i = 0; i < 8; ++i) {
        const unsigned u = pk(xv[2 * i], xv[2 * i + 1]);
        xv[2 * i] = blo(u); xv[2 * i + 1] = bhi(u);
        s1 += xv[2 * i] + xv[2 * i + 1];
        s2 = fmaf(xv[2 * i], xv[2 * i], fmaf(xv[2 * i + 1], xv[2 * i + 1], s2));
      }
      s1 = rowsum4(s1); s2 = rowsum4(s2);
      const int sgb = WS_SG + (tb * 4 + w) * 2048;
      v4u gr = {f2u(s1), tag32, f2u(s2), tag32};
      if (q4 == 0) st128(gr, rw, l16, sgb + cb * 256);
      v4u g0, g1;
#pragma unroll 1
      for (unsigned spins = 0; spins < SPIN_CAP; ++spins) {
        g0 = ld128(rw, l16, sgb);
        g1 = ld128(rw, l16, sgb + 1024);
        asm volatile("" ::: "memory");
        if (__all(g0.y == tag32 && g0.w == tag32 && g1.y == tag32 && g1.w == tag32)) break;
      }
      const float t1 = rowsum4(u2f(g0.x) + u2f(g1.x));
      const float t2 = rowsum4(u2f(g0.z) + u2f(g1.z));
      const float mu = t1 * (1.f / D_);
      const float r = __builtin_amdgcn_rsqf(fmaxf(t2 * (1.f / D_) - mu * mu, 0.f) + EPS);
      STAMP(17);
      gate_publish(xv, mu, r, WS_X3, WS_ST3);
      STAMP(18);
      publish(flags + (4 * 256 + P * 16 + m) * 16, tag, tid);
      STAMP(19);
    } else {
      GAS v4u* o = (GAS v4u*)(p.out + tok * D_ + 64 * cb + 16 * q4);
#pragma unroll
      for (int e = 0; e < 2; ++e) o[e] = v4u{pk(xv[8 * e], xv[8 * e + 1]), pk(xv[8 * e + 2], xv[8 * e + 3]), pk(xv[8 * e + 4], xv[8 * e + 5]), pk(xv[8 * e + 6], xv[8 * e + 7])};
    }
  }
  if (b == 0 && tid == 0) __hip_atomic_store((gu64*)(p.ws + WS_GEN), tag, RLX_AGENT);
  STAMP(25);
}

extern "C" void kernel_launch(void* const* d_in, const int* in_sizes, int n_in, void* d_out, int out_size, void* d_ws, size_t ws_size,
                              hipStream_t stream) {
  Params p;
  p.q = (const unsigned short*)d_in[0]; p.k = (const unsigned short*)d_in[1]; p.v = (const unsigned short*)d_in[2];
  p.sk = (const unsigned short*)d_in[3]; p.hk = (const unsigned short*)d_in[4]; p.hD = (const unsigned short*)d_in[5];
  p.w1 = (const unsigned short*)d_in[7]; p.b1 = (const unsigned short*)d_in[8]; p.w2 = (const unsigned short*)d_in[9];
  p.b2 = (const unsigned short*)d_in[10];
  p.out = (unsigned short*)d_out; p.ws = (unsigned char*)d_ws;
  (void)hipFuncSetAttribute((const void*)h3_mixer_kernel, hipFuncAttributeMaxDynamicSharedMemorySize, LDS_BYTES);
  hipLaunchKernelGGL(h3_mixer_kernel, dim3(256), dim3(256), LDS_BYTES, stream, p);
}
```

```cpp
#include <hip/hip_runtime.h>
#include <hip/hip_bf16.h>
#include <stdint.h>
#include <type_traits>

#define GAS __attribute__((address_space(1)))
#define LAS __attribute__((address_space(3)))
#define CAS __attribute__((address_space(4)))
typedef unsigned v4u __attribute__((ext_vector_type(4)));
typedef unsigned v2u __attribute__((ext_vector_type(2)));
typedef float f32x2 __attribute__((ext_vector_type(2)));
typedef float f32x4 __attribute__((ext_vector_type(4)));
typedef float f32x16 __attribute__((ext_vector_type(16)));
typedef __bf16 bf16x8 __attribute__((ext_vector_type(8)));
typedef __bf16 bf16x2 __attribute__((ext_vector_type(2)));
typedef short s16x4 __attribute__((ext_vector_type(4)));
typedef GAS unsigned long long gu64;
typedef __amdgpu_buffer_rsrc_t rsrc_t;
#define RLX_AGENT __ATOMIC_RELAXED, __HIP_MEMORY_SCOPE_AGENT
#define SC1 16
#define SCHED() __builtin_amdgcn_sched_barrier(0)

#define G2WIN 4
#define G1WIN 4
namespace {
constexpr int S_ = 2048, D_ = 512, F_ = 1792;
constexpr int WS_GEN = 0;
constexpr int WS_FLAG = 4096;
constexpr int FLAG_ARR = 256 * 128;
constexpr int WS_SG = WS_FLAG + 6 * FLAG_ARR;
constexpr int WS_ST1 = WS_SG + 32 * 4 * 8 * 256;
constexpr int WS_ST3 = WS_ST1 + 128 * 8 * 128;
constexpr int WS_QS = WS_ST3 + 128 * 8 * 128;
constexpr int WS_U = 1 << 20;
constexpr int WS_YT = WS_U + (4 << 20);
constexpr int WS_X1 = WS_YT + (2 << 20);
constexpr int WS_X3 = WS_X1 + (2 << 20);
constexpr int WS_H1 = WS_X3 + (2 << 20);
constexpr int WS_H2 = WS_H1 + 128 * 56 * 1024;
constexpr int WS_STAMP = WS_H2 + 128 * 56 * 1024;
constexpr int WS_WF1 = WS_STAMP + (1 << 20);
constexpr int WS_WF2 = WS_WF1 + (2 << 20);
constexpr int WS_END = WS_WF2 + (2 << 20);
static_assert(WS_QS + 2048 * 8 <= WS_U, "ws map");
constexpr int LDS_BYTES = 163840;
constexpr int L_W1 = LDS_BYTES - 114688;
constexpr int L_TAB = 32768;
constexpr int L_B2 = 36352;
constexpr int L_RED = 0;
constexpr int L_YT = 0;
constexpr int L_H = 0;
constexpr int H_CS = 4416, H_CH = 4 * H_CS;
constexpr int L_U = 35328;
constexpr int U_CS = 5120;
constexpr int L_CX = 0;
constexpr int L_WR = 65536;
constexpr int L_UT = 90112;
constexpr int L_TR = 36864;
constexpr float EPS = 1e-5f;
constexpr unsigned SPIN_CAP = 1u << 16;

struct Params {
  const unsigned short *q, *k, *v, *sk, *hk, *hD, *w1, *b1, *w2, *b2;
  unsigned short* out;
  unsigned char* ws;
};

template <int B, int E, class Fn> __device__ __forceinline__ void sfor(Fn&& f) {
  if constexpr (B < E) { f(std::integral_constant<int, B>{}); sfor<B + 1, E>(f); }
}
__device__ __forceinline__ f32x4 sel(int c, f32x4 a, f32x4 b) { return c ? a : b; }
__device__ __forceinline__ float u2f(unsigned x) { return __builtin_bit_cast(float, x); }
__device__ __forceinline__ unsigned f2u(float x) { return __builtin_bit_cast(unsigned, x); }
__device__ __forceinline__ float blo(unsigned x) { return __builtin_bit_cast(float, x << 16); }
__device__ __forceinline__ float bhi(unsigned x) { return __builtin_bit_cast(float, x & 0xffff0000u); }
__device__ __forceinline__ unsigned pk(float a, float b) { bf16x2 r = {(__bf16)a, (__bf16)b}; return __builtin_bit_cast(unsigned, r); }
__device__ __forceinline__ void MF16(f32x4& acc, const v4u& a, const v4u& b) { asm volatile("v_mfma_f32_16x16x32_bf16 %0, %1, %2, %0" : "+v"(acc) : "v"(a), "v"(b)); }
__device__ __forceinline__ void MF16Z(f32x4& acc, const v4u& a, const v4u& b) { asm volatile("v_mfma_f32_16x16x32_bf16 %0, %1, %2, 0" : "=&v"(acc) : "v"(a), "v"(b)); }
__device__ __forceinline__ void MF16A(f32x4& acc, const v4u& a, const v4u& b) { asm volatile("v_mfma_f32_16x16x32_bf16 %0, %1, %2, %0" : "+v"(acc) : "a"(a), "v"(b)); }
__device__ __forceinline__ void MF16A0(f32x4& acc, const v4u& a, const v4u& b) { asm volatile("v_mfma_f32_16x16x32_bf16 %0, %1, %2, 0" : "=&v"(acc) : "a"(a), "v"(b)); }
#define NOPS(n) do { SCHED(); asm volatile("s_nop " #n); SCHED(); } while (0)
__device__ __forceinline__ void acc2(unsigned u, float& s1, float& s2) {
  const bf16x2 v = __builtin_bit_cast(bf16x2, u), one = __builtin_bit_cast(bf16x2, 0x3f803f80u);
  s1 = __builtin_amdgcn_fdot2_f32_bf16(v, one, s1, false);
  s2 = __builtin_amdgcn_fdot2_f32_bf16(v, v, s2, false);
}
__device__ __forceinline__ rsrc_t mk(const void* p, int bytes) { return __builtin_amdgcn_make_buffer_rsrc((void*)p, 0, bytes, 0x00020000); }
__device__ __forceinline__ v4u ld128(rsrc_t r, int voff, int soff) { return __builtin_amdgcn_raw_buffer_load_b128(r, voff, soff, SC1); }
__device__ __forceinline__ void st128(v4u v, rsrc_t r, int voff, int soff) { __builtin_amdgcn_raw_buffer_store_b128(v, r, voff, soff, SC1); }
__device__ __forceinline__ float rowsum4(float x) {
  float a = x, c = x;
  asm("s_nop 1\n\tv_permlane16_swap_b32 %0, %1" : "+v"(a), "+v"(c));
  a += c; c = a;
  asm("s_nop 1\n\tv_permlane32_swap_b32 %0, %1" : "+v"(a), "+v"(c));
  return a + c;
}
__device__ __forceinline__ float sum8(float x) {
  x += u2f(__builtin_amdgcn_update_dpp(0u, f2u(x), 0xB1, 0xf, 0xf, true));
  x += u2f(__builtin_amdgcn_update_dpp(0u, f2u(x), 0x4E, 0xf, 0xf, true));
  x += u2f(__builtin_amdgcn_update_dpp(0u, f2u(x), 0x141, 0xf, 0xf, true));
  return x;
}
#define DRAIN() asm volatile("s_waitcnt vmcnt(0)" ::: "memory")
#define BAR() do { asm volatile("s_waitcnt lgkmcnt(0)" ::: "memory"); __builtin_amdgcn_s_barrier(); asm volatile("" ::: "memory"); } while (0)
#define STAMP(i)

__device__ __forceinline__ void setm0(unsigned x) { asm volatile("s_mov_b32 m0, %0" :: "s"(x) : "memory"); }
template <int IMM> __device__ __forceinline__ void dma16i(rsrc_t r, int voff, int soff) {
  asm volatile("buffer_load_dwordx4 %0, %1, %2 offen offset:%3 sc1 lds" :: "v"(voff), "s"(r), "s"(soff), "i"(IMM) : "memory");
}
template <int IMM> __device__ __forceinline__ void ldv128(v4u& d, rsrc_t r, int voff) {
  asm volatile("buffer_load_dwordx4 %0, %1, %2, 0 offen offset:%3" : "=v"(d) : "v"(voff), "s"(r), "i"(IMM));
}
__device__ __forceinline__ void lda128x4_if0(v4u& d0, v4u& d1, v4u& d2, v4u& d3, rsrc_t r, int voff, int soff, int cond) {
  asm volatile("s_cmp_lg_u32 %7, 0\n\ts_cbranch_scc1 1f\n\t"
               "buffer_load_dwordx4 %0, %4, %5, %6 offen sc1\n\tbuffer_load_dwordx4 %1, %4, %5, %6 offen offset:1024 sc1\n\t"
               "buffer_load_dwordx4 %2, %4, %5, %6 offen offset:2048 sc1\n\tbuffer_load_dwordx4 %3, %4, %5, %6 offen offset:3072 sc1\n1:"
               : "+a"(d0), "+a"(d1), "+a"(d2), "+a"(d3) : "v"(voff), "s"(r), "s"(soff), "s"(cond) : "scc");
}
__device__ __forceinline__ void pin4(v4u& x) { asm volatile("" : "+v"(x)); }
__device__ __forceinline__ void pinf(float& x) { asm volatile("" : "+v"(x)); }
__device__ __forceinline__ void dma16(rsrc_t r, int voff, int soff, unsigned lds_dst) {
  asm volatile("s_mov_b32 m0, %2\n\ts_nop 0\n\tbuffer_load_dwordx4 %0, %1, %3 offen lds" :: "v"(voff), "s"(r), "s"(lds_dst), "s"(soff) : "memory");
}
__device__ __forceinline__ void dma16c(rsrc_t r, int voff, int soff, unsigned lds_dst) {
  asm volatile("s_mov_b32 m0, %2\n\ts_nop 0\n\tbuffer_load_dwordx4 %0, %1, %3 offen sc1 lds" :: "v"(voff), "s"(r), "s"(lds_dst), "s"(soff) : "memory");
}
#define LDV32(d, r, voff) asm volatile("buffer_load_dword %0, %1, %2, 0 offen" : "=v"(d) : "v"(voff), "s"(r))
#define LDV16(d, r, voff) asm volatile("buffer_load_ushort %0, %1, %2, 0 offen" : "=v"(d) : "v"(voff), "s"(r))
#define LDV128(d, r, voff, imm) asm volatile("buffer_load_dwordx4 %0, %1, %2, 0 offen offset:%3" : "=v"(d) : "v"(voff), "s"(r), "i"(imm))
#define LDA128(d, r, voff, soff, imm) asm volatile("buffer_load_dwordx4 %0, %1, %2, %3 offen offset:%4 sc1" : "=a"(d) : "v"(voff), "s"(r), "s"(soff), "i"(imm))
#define WAITV(n) asm volatile("s_waitcnt vmcnt(%0)" :: "i"(n) : "memory")
#define PIN(x) asm volatile("" : "+v"(x))
#define PINS(x) asm volatile("" : "+s"(x))
#define LDP(base, i) ld128(rw, l16 + ((i) & 3) * 1024, (base) + ((i) & ~3) * 1024)
__device__ __forceinline__ v2u trd(LAS unsigned char* a) { return __builtin_bit_cast(v2u, __builtin_amdgcn_ds_read_tr16_b64_v4i16((LAS s16x4*)a)); }
__device__ __forceinline__ int usw(int y) { return ((y >> 1) & 3) << 1; }
__device__ __forceinline__ void publish(gu64* flag, unsigned long long tag, int tid) {
  DRAIN();
  BAR();
  if (tid == 0) __hip_atomic_store(flag, tag, RLX_AGENT);
}
__device__ __forceinline__ void await(gu64* base, int idx, unsigned long long tag, int w) {
  if (w == 0) {
    gu64* f = base + idx * 16;
#pragma unroll 1
    for (unsigned spins = 0; spins < SPIN_CAP; ++spins) {
      unsigned long long x = __hip_atomic_load(f, RLX_AGENT);
      if (__all(x == tag)) break;
      __builtin_amdgcn_s_sleep(1);
    }
  }
  __builtin_amdgcn_fence(__ATOMIC_ACQUIRE, "wavefront");
  BAR();
}
}

__global__ void __launch_bounds__(256, 1) h3_mixer_kernel(Params p) {
  extern __shared__ __attribute__((aligned(16))) unsigned char smem[];
  LAS unsigned char* L = (LAS unsigned char*)smem;
  const unsigned Lb = (unsigned)(uintptr_t)L;
  const int tid = threadIdx.x, lane = tid & 63, w = __builtin_amdgcn_readfirstlane(tid >> 6);
  const int b = blockIdx.x;
  const int P = (b & 7) + 8 * (b >> 7), m = (b >> 3) & 15;
  const int th = m >> 3, cb = m & 7, tb = 2 * P + th;
  const int s = lane & 15, q4 = lane >> 4, l16 = lane * 16;
  const rsrc_t rw = mk(p.ws, WS_END);
  STAMP(0);
  gu64* flags = (gu64*)(p.ws + WS_FLAG);
  const GAS unsigned char* code0;
  int ncode;
  asm volatile("s_getpc_b64 %0\n.Lh3_code_begin:\n\ts_mov_b32 %1, .Lh3_code_end-.Lh3_code_begin" : "=s"(code0), "=s"(ncode));
  code0 += ((b >> 3) & 7) * 128; ncode -= 1024;
  v2u gen;
  asm volatile("buffer_load_dwordx2 %0, off, %1, 0 sc1" : "=v"(gen) : "s"(rw));

  const int cp = 32 * cb + tb;
  unsigned touch;
  {
    const rsrc_t rk = mk(p.k, S_ * D_ * 2), rv = mk(p.v, S_ * D_ * 2), rs = mk(p.sk, D_ * 8);
    const int g = 2 * w + (lane >> 5), cpl = lane & 31;
    const int t0 = 64 * tb + 8 * g, ch = 64 * cb + 2 * cpl;
    unsigned kr[11], vr[8];
    v4u sk;
#pragma unroll
    for (int i = 0; i < 11; ++i) { const int o = (t0 - 3 + i) * 1024 + ch * 2; LDV32(kr[i], rk, o); if (i >= 3) LDV32(vr[i - 3], rv, o); }
    LDV128(sk, rs, ch * 8, 0);
    {
      const int s8 = lane >> 3;
      const GAS unsigned char* a = w == 0 ? code0 + (lane * 128 * 8 < ncode ? lane * 128 * 8 : 0)
                                 : w == 1 ? (const GAS unsigned char*)p.hk + 2 * cp * 4096 + lane * 128
                                 : w == 2 ? (const GAS unsigned char*)p.q + (64 * tb + lane) * 1024 + 128 * cb
                                          : (const GAS unsigned char*)p.q + (64 * tb + 8 * cb + s8) * 1024 + 128 * (lane & 7);
      asm volatile("global_load_dword %0, %1, off" : "=v"(touch) : "v"(a));
    }
    const unsigned wr = Lb + L_WR + w * 6144;
    {
      const rsrc_t r1 = mk(p.w1, D_ * F_ * 2), r2 = mk(p.w2, F_ * D_ * 2);
      int o1 = (16 * q4 + ((lane >> 2) & 3)) * (F_ * 2) + 64 * w + 16 * (lane & 3);
      if (w == 3 && (lane & 2)) o1 = 0x7ffffff0;
      const int so1 = (64 * (P >> 1) + 8 * (P & 1)) * (F_ * 2) + 224 * m;
      dma16(r1, o1, so1, wr);
      dma16(r1, o1, so1 + 4 * (F_ * 2), wr + 1024);
      const int k2 = 2 * tb + (w >> 1), pr = k2 / 7, j = k2 - 7 * pr;
      const int kq = (lane >> 5), qq = (lane >> 3) & 3;
      int o2 = (j == 3 ? 8 * kq + qq : 4 * kq + qq) * (D_ * 2) + 128 * cb + 16 * (lane & 7);
      if (tb >= 28) o2 = 0x7ffffff0;
      const int f0 = 224 * pr + (j == 3 ? 96 : (j > 3 ? 112 + 32 * (j - 4) : 32 * j));
      const int dh = (j == 3 ? 4 : 16), dk = (j == 3 ? 112 : 8);
#pragma unroll
      for (int i = 0; i < 4; ++i) dma16(r2, o2, (f0 + dh * (i >> 1) + dk * (i & 1)) * (D_ * 2), wr + 2048 + i * 1024);
    }
    asm volatile("s_waitcnt vmcnt(7)" : "+v"(gen), "+v"(kr[0]), "+v"(kr[1]), "+v"(kr[2]), "+v"(kr[3]), "+v"(kr[4]), "+v"(kr[5]), "+v"(kr[6]), "+v"(kr[7]), "+v"(kr[8]),
                 "+v"(kr[9]), "+v"(kr[10]), "+v"(vr[0]), "+v"(vr[1]), "+v"(vr[2]), "+v"(vr[3]), "+v"(vr[4]), "+v"(vr[5]), "+v"(vr[6]), "+v"(vr[7]), "+v"(sk));
    const f32x2 s0 = {blo(sk.x), blo(sk.z)}, s1 = {bhi(sk.x), bhi(sk.z)}, s2 = {blo(sk.y), blo(sk.w)}, s3 = {bhi(sk.y), bhi(sk.w)};
    f32x2 kf[11], uu[8];
#pragma unroll
    for (int i = 0; i < 11; ++i) kf[i] = f32x2{blo(kr[i]), bhi(kr[i])};
#pragma unroll
    for (int i = 0; i < 8; ++i) {
      f32x2 a = kf[i] * s3;
      a = kf[i + 1] * s2 + a;
      a = kf[i + 2] * s1 + a;
      a = kf[i + 3] * s0 + a;
      uu[i] = a * f32x2{blo(vr[i]), bhi(vr[i])};
    }
#pragma unroll
    for (int c = 0; c < 2; ++c)
      *(LAS v4u*)(L + L_UT + (2 * cpl + c) * 128 + 16 * ((7 - g) ^ (cpl & 7))) = v4u{pk(uu[7][c], uu[6][c]), pk(uu[5][c], uu[4][c]), pk(uu[3][c], uu[2][c]), pk(uu[1][c], uu[0][c])};
    BAR();
#pragma unroll
    for (int i = 0; i < 2; ++i) {
      const int r = 16 * w + 8 * i + (lane >> 3);
      st128(*(LAS v4u*)(L + L_UT + r * 128 + 16 * ((lane & 7) ^ ((r >> 1) & 7))), rw, (lane >> 3) * 4096 + (lane & 7) * 16, WS_U + (64 * cb + 16 * w + 8 * i) * 4096 + (31 - tb) * 128);
    }
    SCHED();
    WAITV(2);
    LAS unsigned char* a1 = L + L_WR + w * 6144 + (lane >> 2) * 64 + 8 * (lane & 3);
    LAS unsigned char* a2 = L + L_WR + w * 6144 + 2048 + (lane >> 2) * 128 + 32 * (lane & 3) + 16 * (w & 1);
#pragma unroll
    for (int e = 0; e < 2; ++e) {
      const v2u lo = trd(a1 + 32 * e), hi = trd(a1 + 1024 + 32 * e);
      if (2 * w + e < 7) st128(v4u{lo.x, lo.y, hi.x, hi.y}, rw, l16, WS_WF1 + ((m * 16 + P) * 7 + 2 * w + e) * 1024);
    }
#pragma unroll
    for (int e = 0; e < 2; ++e) {
      const v2u lo = trd(a2 + 8 * e), hi = trd(a2 + 2048 + 8 * e);
      if (tb < 28) st128(v4u{lo.x, lo.y, hi.x, hi.y}, rw, l16, WS_WF2 + ((cb * 56 + 2 * tb) * 4 + 2 * w + e) * 1024);
    }
  }
  const unsigned long long tag = (unsigned)__builtin_amdgcn_readfirstlane(gen.x) + 1ull + ((unsigned long long)(unsigned)__builtin_amdgcn_readfirstlane(gen.y) << 32);
  const unsigned tag32 = (unsigned)tag;
  {
    v4u z = {0u, ~tag32, 0u, ~tag32};
    if (q4 == 0) st128(z, rw, l16, WS_SG + ((tb * 4 + w) * 8 + cb) * 256);
  }
  STAMP(1);
  publish(flags + (0 * 256 + cp) * 16, tag, tid);
  STAMP(2);

  PIN(touch);
  v4u qrow[8];
  unsigned ub1[2], ub2;
  const int tok = 64 * tb + 32 * ((s >> 2) & 1) + 8 * w + 2 * (s & 3) + (s >> 3);
  *(LAS v4u*)(L + L_U + w * U_CS + 4096 + l16) = v4u{0u, 0u, 0u, 0u};
  const unsigned uD = *(const CAS unsigned*)(p.hD + 2 * cp);
  const float dD = (w >> 1) ? bhi(uD) : blo(uD);
  {
    const rsrc_t rh = mk(p.hk, D_ * S_ * 2), rq_ = mk(p.q, S_ * D_ * 2);
    if (w == 3) {
      const int vo = (64 * tb + 8 * cb + (lane >> 3)) * 1024 + 16 * (lane & 7);
      sfor<0, 8>([&](auto ec) { constexpr int e = ec; ldv128<128 * e>(qrow[e], rq_, vo); });
    }
    if (w != 0) {
#pragma unroll
      for (int e = 0; e < 3; ++e) {
        const int cc = w - 1 + 3 * e;
        if (cc < 8) {
          const int vo = l16 + 4 * (cc & 3) - 12;
          setm0(Lb + L_H + cc * H_CS + 7 * 16);
          asm volatile("s_nop 0\n\tbuffer_load_dwordx4 %0, %1, %2 offen lds\n\tbuffer_load_dwordx4 %0, %1, %2 offen offset:1024 lds\n\t"
                       "buffer_load_dwordx4 %0, %1, %2 offen offset:2048 lds\n\tbuffer_load_dwordx4 %0, %1, %2 offen offset:3072 lds"
                       :: "v"(vo), "s"(rh), "s"((2 * cp + (cc >> 2)) * 4096) : "memory");
        }
      }
      const rsrc_t rb1 = mk(p.b1, F_ * 2), rb2 = mk(p.b2, D_ * 2);
      LDV16(ub1[0], rb1, (112 * m + 16 * (w == 3 ? 0 : 2 * w + 1) + s) * 2 );
      LDV16(ub1[1], rb1, (112 * m + 16 * (w == 3 ? 1 : 2 * w + 2) + s) * 2);
      LDV16(ub2, w == 3 ? rb1 : rb2, w == 3 ? (112 * m + 32 + s) * 2 : (64 * cb + lane) * 2);
    }
    if (w == 1 || w == 2) {
      const int s8 = lane >> 3, vo = (32 * (s8 >> 2) + 2 * (s8 & 3)) * 1024 + 16 * (lane & 7);
#pragma unroll
      for (int e = 0; e < 4; ++e) dma16(rq_, vo, (64 * tb + 8 * (2 * (w & 1) + (e >> 1)) + (e & 1)) * 1024 + 128 * cb, Lb + L_WR + (4 * (w & 1) + e) * 1024);
    }
    if (w == 3) {
      asm volatile("s_waitcnt vmcnt(11)" : "+v"(qrow[0]), "+v"(qrow[1]), "+v"(qrow[2]), "+v"(qrow[3]), "+v"(qrow[4]), "+v"(qrow[5]), "+v"(qrow[6]), "+v"(qrow[7]));
      float s1 = 0.f, s2 = 0.f;
#pragma unroll
      for (int e = 0; e < 8; ++e)
#pragma unroll
        for (int i = 0; i < 4; ++i) acc2(qrow[e][i], s1, s2);
      s1 = sum8(s1); s2 = sum8(s2);
      const float mu = s1 * (1.f / D_), rs = __builtin_amdgcn_rsqf(fmaxf(s2 * (1.f / D_) - mu * mu, 0.f) + EPS);
      if ((lane & 7) == 0) __builtin_amdgcn_raw_buffer_store_b64(v2u{f2u(mu), f2u(rs)}, rw, lane, WS_QS + (64 * tb + 8 * cb) * 8, SC1);
    }
  }
  STAMP(3);
  await(flags + (0 * 256) * 16, cb * 32 + (lane & 31), tag, w);
  STAMP(4);
  {
    v4u ut[2];
    unsigned nx[2];
    const int us = WS_U + (2 * cp + (w >> 1)) * 4096 + (w & 1) * 2048;
#pragma unroll
    for (int i = 0; i < 2; ++i) { ut[i] = ld128(rw, l16, us + i * 1024); nx[i] = __builtin_amdgcn_raw_buffer_load_b32(rw, l16 + 16, us + i * 1024, SC1); }
    if ((w & 1) && lane == 63) nx[1] = 0u;
#pragma unroll
    for (int i = 0; i < 2; ++i) {
      const int y = 16 * (w & 1) + 8 * i + (lane >> 3);
      LAS unsigned char* d = L + L_U + (w >> 1) * 2 * U_CS + y * 128 + 16 * ((lane & 7) ^ usw(y));
      *(LAS v4u*)(d + U_CS) = ut[i];
      *(LAS v4u*)(d) = v4u{__builtin_amdgcn_alignbit(ut[i].y, ut[i].x, 16), __builtin_amdgcn_alignbit(ut[i].z, ut[i].y, 16),
                           __builtin_amdgcn_alignbit(ut[i].w, ut[i].z, 16), __builtin_amdgcn_alignbit(nx[i], ut[i].w, 16)};
    }
  }
  DRAIN();
  if (w != 0) {
    asm volatile("" : "+v"(ub1[0]), "+v"(ub1[1]), "+v"(ub2));
    const int cc = w - 1 + 3 * (lane >> 3), c = cc & 3;
    const v4u ha = *(const CAS v4u*)(p.hk + 2 * cp * S_), hb_ = *(const CAS v4u*)(p.hk + (2 * cp + 1) * S_);
    const unsigned h0 = cc < 4 ? ha.x : hb_.x, h1 = cc < 4 ? ha.y : hb_.y, h2 = cc < 4 ? ha.z : hb_.z;
    v4u o = {0u, c == 2 ? h0 : 0u, c == 2 ? h1 : (c == 1 ? h0 : 0u), c == 2 ? h2 : (c == 1 ? h1 : h0)};
    if ((lane & 7) != 7) o = v4u{0u, 0u, 0u, 0u};
    if (lane < 24 && cc < 8 && !(c == 3 && (lane & 7) == 7)) *(LAS v4u*)(L + L_H + cc * H_CS + (lane & 7) * 16) = o;
  }
  BAR();
  STAMP(5);
  v4u xr[2];
  xr[0] = *(LAS v4u*)(L + L_WR + w * 2048 + s * 128 + 32 * q4);
  xr[1] = *(LAS v4u*)(L + L_WR + w * 2048 + s * 128 + 32 * q4 + 16);
  {
    const int chl = w >> 1, kh = w & 1, pp = s >> 3, a_ = s & 7;
    LAS unsigned char* Ab = L + L_H + chl * H_CH + (s & 3) * H_CS + 16 * ((s >> 2) + q4) + kh * 512;
    const int ub = L_U + (chl * 2 + pp) * U_CS, Y0 = 7 - a_ + 4 * kh;
    const int w1s = WS_WF1 + m * 114688 + (7 + 26 * w) * 1024;
    const unsigned w1d = Lb + L_W1 + (7 + 26 * w) * 1024;
    f32x4 acc[4][2];
    v4u A[3][4], B[2][4];
    auto ldA = [&](int kap, v4u (&f)[4]) {
#pragma unroll
      for (int c = 0; c < 4; ++c) f[c] = *(LAS v4u*)(Ab + 1024 * c + 64 * kap);
    };
    auto ldB = [&](int ii, v4u (&f)[4]) {
      const int yy = Y0 + (ii >> 1);
      LAS unsigned char* bp = L + (ub + yy * 128 + 16 * ((4 * (ii & 1) + q4) ^ usw(yy)));
#pragma unroll
      for (int j = 0; j < 4; ++j) f[j] = *(LAS v4u*)(bp + (3 - j) * 1024);
    };
    ldB(0, B[0]); ldA(0, A[0]); ldA(1, A[1]);
    PIN(xr[0]); PIN(xr[1]);
    BAR();
    sfor<0, 8>([&](auto iic) {
      constexpr int ii = iic;
      if constexpr (ii + 1 < 8) { ldB(ii + 1, B[(ii + 1) & 1]); ldA(ii + 2, A[(ii + 2) % 3]); }
      SCHED();
      sfor<0, 20>([&](auto nc) {
        constexpr int n = nc, pr = n >> 1, I = n & 1;
        constexpr int c = pr < 4 ? 0 : (pr < 7 ? 1 : (pr < 9 ? 2 : 3)), J = pr < 4 ? pr : (pr < 7 ? pr - 3 : (pr < 9 ? pr - 5 : 3));
        constexpr int vm = (n % 5 == 0) ? ii * 4 + n / 5 : 99;
        if constexpr (vm < 26 && vm % 4 == 0) setm0(w1d + vm * 1024);
        if constexpr (ii == 0 && c == 0) MF16Z(acc[J][I], A[(ii + I) % 3][c], B[ii & 1][J - c]);
        else MF16(acc[J][I], A[(ii + I) % 3][c], B[ii & 1][J - c]);
        if constexpr (vm < 26) dma16i<(vm & 3) * 1024>(rw, l16, w1s + (vm & ~3) * 1024);
      });
      SCHED();
    });
    NOPS(7);
    STAMP(29);
    LAS f32x4* cx = (LAS f32x4*)(L + L_CX);
    BAR();
#pragma unroll
    for (int g = 0; g < 4; ++g) cx[(w * 4 + g) * 64 + lane] = sel(kh, acc[g >> 1][g & 1], acc[2 + (g >> 1)][g & 1]);
    BAR();
#pragma unroll
    for (int jj = 0; jj < 2; ++jj) {
      const int a = 16 * kh + 8 * jj + a_, yr = 31 - a;
      v4u o;
#pragma unroll
      for (int I = 0; I < 2; ++I) {
        const f32x4 ot = cx[((w ^ 1) * 4 + 2 * jj + I) * 64 + lane];
        const v4u uc = *(LAS v4u*)(L + ub + yr * 128 + 16 * ((7 - 4 * I - q4) ^ usw(yr)));
        const f32x4 mine = sel(kh, acc[2 + jj][I], acc[jj][I]);
        float yv[4];
#pragma unroll
        for (int r = 0; r < 4; ++r) yv[r] = mine[r] + ot[r] + blo(uc[3 - r]) * dD;
        o[2 * I] = pk(yv[0], yv[1]); o[2 * I + 1] = pk(yv[2], yv[3]);
      }
      *(LAS v4u*)(L + L_CX + 16384 + w * 2048 + jj * 1024 + a_ * 128 + 16 * ((2 * q4 + pp) ^ a_)) = o;
    }
#pragma unroll
    for (int jj = 0; jj < 2; ++jj)
      st128(*(LAS v4u*)(L + L_CX + 16384 + w * 2048 + jj * 1024 + (lane >> 3) * 128 + 16 * ((lane & 7) ^ (lane >> 3))), rw, (lane >> 3) * 65536 + (lane & 7) * 16,
            WS_YT + ((16 * kh + 8 * jj) * 512 + 2 * cp + chl) * 128);
  }
  STAMP(6);
  publish(flags + (1 * 256 + cp) * 16, tag, tid);
  STAMP(7);

  {
    const int pc0 = 2 * w, pc1 = w == 3 ? 111 : 2 * w + 1;
    dma16c(rw, l16, WS_WF1 + m * 114688 + pc0 * 1024, Lb + L_W1 + pc0 * 1024);
    dma16c(rw, l16, WS_WF1 + m * 114688 + pc1 * 1024, Lb + L_W1 + pc1 * 1024);
  }
  v4u w2f[14][4];
  const int w2s = WS_WF2 + (cb * 56 + 14 * w) * 4096;
#pragma unroll
  for (int kk = 0; kk < 3; ++kk)
#pragma unroll
    for (int jt = 0; jt < 4; ++jt) LDA128(w2f[kk][jt], rw, l16, w2s + kk * 4096, jt * 1024);
  STAMP(8);
  await(flags + (1 * 256) * 16, lane < 32 ? cb * 32 + lane : (lane & 7) * 32 + tb, tag, w);
  STAMP(9);
  v2u qs;
  unsigned yp[8];
  {
    v4u yt[2];
    qs = __builtin_amdgcn_raw_buffer_load_b64(rw, tok * 8, WS_QS, SC1);
#pragma unroll
    for (int i = 0; i < 2; ++i) yt[i] = ld128(rw, l16, WS_YT + (tb * 512 + 64 * cb) * 128 + (w * 2 + i) * 1024);
#pragma unroll
    for (int i = 0; i < 2; ++i) *(LAS v4u*)(L + L_YT + (w * 2 + i) * 1024 + lane * 16) = yt[i];
    BAR();
#pragma unroll
    for (int s4 = 0; s4 < 4; ++s4) {
      const v2u r = trd(L + L_YT + (16 * q4 + 4 * s4 + (s >> 2)) * 128 + 32 * w + 8 * (s & 3));
      yp[2 * s4] = r.x; yp[2 * s4 + 1] = r.y;
    }
  }
  auto gate_publish = [&](const float (&x)[16], float mu, float r, int xoff, int stoff) {
    float s1 = 0.f, s2 = 0.f;
#pragma unroll
    for (int i = 0; i < 8; ++i) {
      asm volatile("" : "+v"(yp[i]));
      const float a0 = x[2 * i] + (x[2 * i] - mu) * r * blo(yp[i]), a1 = x[2 * i + 1] + (x[2 * i + 1] - mu) * r * bhi(yp[i]);
      const unsigned u = pk(a0, a1);
      xr[i >> 2][i & 3] = u;
      acc2(u, s1, s2);
    }
#pragma unroll
    for (int e = 0; e < 2; ++e) st128(xr[e], rw, l16, xoff + ((tb * 4 + w) * 16 + 2 * cb + e) * 1024);
    s1 = rowsum4(s1); s2 = rowsum4(s2);
    v2u st = {f2u(s1), f2u(s2)};
    if (q4 == 0) __builtin_amdgcn_raw_buffer_store_b64(st, rw, s * 8, stoff + ((tb * 4 + w) * 8 + cb) * 128, SC1);
  };
  {
    float x[16];
#pragma unroll
    for (int i = 0; i < 8; ++i) { x[2 * i] = blo(xr[i >> 2][i & 3]); x[2 * i + 1] = bhi(xr[i >> 2][i & 3]); }
    gate_publish(x, u2f(qs.x), u2f(qs.y), WS_X1, WS_ST1);
  }
  STAMP(10);
  publish(flags + (2 * 256 + P * 16 + m) * 16, tag, tid);
  STAMP(11);
#pragma unroll
  for (int kk = 3; kk < 6; ++kk)
#pragma unroll
    for (int jt = 0; jt < 4; ++jt) LDA128(w2f[kk][jt], rw, l16, w2s + kk * 4096, jt * 1024);
#pragma unroll
  for (int kk = 6; kk < 14; ++kk)
#pragma unroll
    for (int jt = 0; jt < 4; ++jt) asm volatile("" : "=a"(w2f[kk][jt]));
  LAS unsigned char* Wa = L + L_W1 + l16;
  auto w1frag = [&](int ks, int ft) { return *(LAS v4u*)(Wa + (ks * 7 + ft) * 1024); };
  if (w != 0) {
    v4u ones = {0x3f803f80u, 0x3f803f80u, 0x3f803f80u, 0x3f803f80u};
    PIN(ones);
    NOPS(1);
#pragma unroll
    for (int fi = 0; fi < 3; ++fi) {
      if (fi < 2 || w == 3) {
        const int ft = w == 3 ? fi : 2 * w + 1 + fi;
        f32x4 c;
        v4u a[16];
#pragma unroll
        for (int ks = 0; ks < 16; ++ks) a[ks] = *(LAS v4u*)(Wa + ft * 1024 + ks * 7168);
        MF16Z(c, ones, a[0]);
#pragma unroll
        for (int ks = 1; ks < 16; ++ks) MF16(c, ones, a[ks]);
        NOPS(7);
        const unsigned hi = pk(c[0], c[0]), lo = pk(c[0] - blo(hi), c[0] - blo(hi)), bb = fi < 2 ? ub1[fi] : ub2;
        v4u o = {hi, lo, bb | (bb << 16), 0u};
        if (q4) o = v4u{0u, 0u, 0u, 0u};
        if (q4 < 2) *(LAS v4u*)(L + L_TAB + q4 * 1792 + ft * 256 + s * 16) = o;
      }
    }
    if (w == 1) *(LAS float*)(L + L_B2 + lane * 4) = blo(ub2);
  }
  LAS unsigned char* We = L + L_TAB + (q4 ? 1792 : 0) + s * 16;

#pragma unroll 1
  for (int layer = 0; layer < 2; ++layer) {
    int xb = (layer ? WS_X3 : WS_X1) + (8 * P + 2 * w) * 16384, stb = (layer ? WS_ST3 : WS_ST1) + (8 * P + 2 * w) * 1024;
    int hb = (layer ? WS_H2 : WS_H1) + ((8 * P + 2 * w) * 56 + 7 * (m >> 1)) * 1024, hb2 = (layer ? WS_H2 : WS_H1) + (tb * 4 * 56 + 14 * w) * 1024, w2l = w2s;
    PINS(xb); PINS(stb); PINS(hb); PINS(hb2); PINS(w2l);
    await(flags + ((layer ? 4 : 2) * 256 + P * 16) * 16, s, tag, w);
    STAMP(12 + 8 * layer);
    {
      constexpr int XW = G1WIN, AW = 5;
      v4u xf[2][XW + 1], af[AW], xe[2];
      v2u st[2][2];
#pragma unroll
      for (int ks = 0; ks < XW; ++ks) {
#pragma unroll
        for (int tt = 0; tt < 2; ++tt) xf[tt][ks] = LDP(xb, tt * 16 + ks);
        if (ks == 1) {
#pragma unroll
          for (int tt = 0; tt < 2; ++tt)
#pragma unroll
            for (int c = 0; c < 2; ++c) st[tt][c] = __builtin_amdgcn_raw_buffer_load_b64(rw, q4 * 256 + s * 8 + c * 128 + tt * 1024, stb, SC1);
        }
        SCHED();
      }
      f32x4 acc[2][7];
      float sg[2], cA[2], cB[2];
#pragma unroll
      for (int n = 0; n < AW - 1; ++n) af[n] = w1frag(0, n);
      SCHED();
      sfor<0, 119>([&](auto nc) {
        constexpr int n = nc, ks = n / 7, ft = n % 7, nn = n + AW - 1;
        if constexpr (nn < 112) af[nn % AW] = w1frag(nn / 7, nn % 7);
        else if constexpr (nn < 119) af[nn % AW] = *(LAS v4u*)(We + (nn - 112) * 256);
        SCHED();
        if constexpr (ks == 0) { MF16Z(acc[0][ft], af[n % AW], xf[0][0]); MF16Z(acc[1][ft], af[n % AW], xf[1][0]); }
        else if constexpr (ks < 16) { MF16(acc[0][ft], af[n % AW], xf[0][ks % (XW + 1)]); MF16(acc[1][ft], af[n % AW], xf[1][ks % (XW + 1)]); }
        else { MF16(acc[0][ft], af[n % AW], xe[0]); MF16(acc[1][ft], af[n % AW], xe[1]); }
        SCHED();
        if constexpr ((ft == 1 || ft == 4) && ks + XW < 16) { xf[ft / 3][(ks + XW) % (XW + 1)] = LDP(xb, (ft / 3) * 16 + ks + XW); SCHED(); }
        if constexpr (ft == 2 && (ks == 4 || ks == 5)) {
          constexpr int tt = ks - 4;
          const float s1 = rowsum4(u2f(st[tt][0].x) + u2f(st[tt][1].x)), s2 = rowsum4(u2f(st[tt][0].y) + u2f(st[tt][1].y));
          const float mu = s1 * (1.f / D_), var = fmaxf(s2 * (1.f / D_) - mu * mu, 0.f);
          const float r1 = __builtin_amdgcn_rsqf(var + EPS);
          const float r = r1 * __builtin_amdgcn_rsqf(var * r1 * r1 + EPS);
          sg[tt] = __builtin_amdgcn_rcpf(r);
          cB[tt] = -2.302208198f * r;
          cA[tt] = cB[tt] * 0.044715f * r * r;
          const unsigned h = pk(-mu, sg[tt]), mh = pk(-mu, -mu - blo(h)), sh = pk(sg[tt], sg[tt] - bhi(h));
          xe[tt] = v4u{q4 ? 0u : mh, q4 ? 0u : mh, q4 ? 0u : sh, 0u};
          pin4(xe[tt]); pinf(sg[tt]); pinf(cA[tt]); pinf(cB[tt]);
          SCHED();
        }
        if constexpr (n == 12 * 7) __builtin_amdgcn_s_waitcnt(0x0f70);
        if constexpr (ft == 3 && ks >= 12 && ks < 16) {
          constexpr int kk = 6 + ks - 12;
          lda128x4_if0(w2f[kk][0], w2f[kk][1], w2f[kk][2], w2f[kk][3], rw, l16, w2l + kk * 4096, layer);
        }
      });
      NOPS(7);
      if (layer == 0) STAMP(27);
      const int hp0 = hb + (m & 1) * 4096;
      auto gelu4 = [&](f32x4 y, int tt, unsigned& o0, unsigned& o1) {
        const f32x4 a = y * (y * y * cA[tt] + cB[tt]);
        f32x4 e;
#pragma unroll
        for (int r = 0; r < 4; ++r) e[r] = __builtin_amdgcn_exp2f(a[r]);
        e = e * sg[tt] + sg[tt];
#pragma unroll
        for (int r = 0; r < 4; ++r) e[r] = __builtin_amdgcn_rcpf(e[r]);
        e = e * y;
        o0 = pk(e[0], e[1]); o1 = pk(e[2], e[3]);
      };
      sfor<0, 6>([&](auto gc) {
        constexpr int g = gc, pr = g >> 1, tt = g & 1;
        unsigned o[4];
        gelu4(acc[tt][2 * pr], tt, o[0], o[1]);
        gelu4(acc[tt][2 * pr + 1], tt, o[2], o[3]);
        st128(v4u{o[0], o[1], o[2], o[3]}, rw, l16 + pr * 1024, hp0 + tt * 56 * 1024);
        SCHED();
        if constexpr (g < 4) lda128x4_if0(w2f[10 + g][0], w2f[10 + g][1], w2f[10 + g][2], w2f[10 + g][3], rw, l16, w2l + (10 + g) * 4096, layer);
      });
      unsigned h6[2][2];
      gelu4(acc[0][6], 0, h6[0][0], h6[0][1]);
      gelu4(acc[1][6], 1, h6[1][0], h6[1][1]);
      asm("s_nop 1\n\tv_permlane16_swap_b32 %0, %2\n\tv_permlane16_swap_b32 %1, %3" : "+v"(h6[0][0]), "+v"(h6[0][1]), "+v"(h6[1][0]), "+v"(h6[1][1]));
      st128(v4u{h6[0][0], h6[0][1], h6[1][0], h6[1][1]}, rw, (q4 & 1) * 56 * 1024 + (q4 >> 1) * 256 + s * 16, hb + 3 * 1024 + (m & 1) * 512);
    }
    STAMP(13 + 8 * layer);
    publish(flags + ((layer ? 5 : 3) * 256 + P * 16 + m) * 16, tag, tid);
    STAMP(14 + 8 * layer);
    await(flags + ((layer ? 5 : 3) * 256 + P * 16) * 16, s, tag, w);
    STAMP(15 + 8 * layer);
    float xv[16];
    {
      constexpr int WIN = G2WIN;
      v4u hf[WIN][4];
#pragma unroll
      for (int kk = 0; kk < WIN; ++kk)
#pragma unroll
        for (int t = 0; t < 4; ++t) hf[kk][t] = LDP(hb2, t * 56 + kk);
      SCHED();
      f32x4 acc[4][4];
#pragma unroll
      for (int kk = 0; kk < 14; ++kk) {
#pragma unroll
        for (int t = 0; t < 4; ++t) {
#pragma unroll
          for (int jt = 0; jt < 4; ++jt) {
            if (kk == 0) MF16A0(acc[t][jt], w2f[kk][jt], hf[kk % WIN][t]);
            else MF16A(acc[t][jt], w2f[kk][jt], hf[kk % WIN][t]);
          }
          SCHED();
          if (kk + WIN < 14) hf[kk % WIN][t] = LDP(hb2, t * 56 + kk + WIN);
          SCHED();
        }
      }
      NOPS(7);
      if (layer == 0) STAMP(28);
      LAS f32x4* red = (LAS f32x4*)(L + L_RED);
      const int hi = w >> 1, lo = w & 1;
      f32x4 k0[4], k1[4];
#pragma unroll
      for (int jt = 0; jt < 4; ++jt) {
        red[((w ^ 2) * 8 + jt) * 64 + lane] = sel(hi, acc[0][jt], acc[2][jt]);
        red[((w ^ 2) * 8 + 4 + jt) * 64 + lane] = sel(hi, acc[1][jt], acc[3][jt]);
        k0[jt] = sel(hi, acc[2][jt], acc[0][jt]);
        k1[jt] = sel(hi, acc[3][jt], acc[1][jt]);
      }
      BAR();
#pragma unroll
      for (int jt = 0; jt < 4; ++jt) { k0[jt] += red[(w * 8 + jt) * 64 + lane]; k1[jt] += red[(w * 8 + 4 + jt) * 64 + lane]; }
      asm volatile("s_waitcnt lgkmcnt(0)" ::: "memory");
#pragma unroll
      for (int jt = 0; jt < 4; ++jt) red[(w * 8 + jt) * 64 + lane] = sel(lo, k0[jt], k1[jt]);
      BAR();
#pragma unroll
      for (int jt = 0; jt < 4; ++jt) {
        const f32x4 o = (sel(lo, k1[jt], k0[jt])) + red[((w ^ 1) * 8 + jt) * 64 + lane];
#pragma unroll
        for (int r = 0; r < 4; ++r) xv[4 * jt + r] = o[r];
      }
#pragma unroll
      for (int i = 0; i < 4; ++i) {
        const f32x4 bb = *(LAS f32x4*)(L + L_B2 + (16 * q4 + 4 * i) * 4);
        xv[4 * i] += bb[0] + blo(xr[i >> 1][2 * (i & 1)]); xv[4 * i + 1] += bb[1] + bhi(xr[i >> 1][2 * (i & 1)]);
        xv[4 * i + 2] += bb[2] + blo(xr[i >> 1][2 * (i & 1) + 1]); xv[4 * i + 3] += bb[3] + bhi(xr[i >> 1][2 * (i & 1) + 1]);
      }
    }
    STAMP(16 + 8 * layer);
    if (layer == 0) {
      float s1 = 0.f, s2 = 0.f;
#pragma unroll
      for (int i = 0; i < 8; ++i) {
        const unsigned u = pk(xv[2 * i], xv[2 * i + 1]);
        xv[2 * i] = blo(u); xv[2 * i + 1] = bhi(u);
        acc2(u, s1, s2);
      }
      s1 = rowsum4(s1); s2 = rowsum4(s2);
      const int sgb = WS_SG + (tb * 4 + w) * 2048;
      v4u gr = {f2u(s1), tag32, f2u(s2), tag32};
      if (q4 == 0) st128(gr, rw, l16, sgb + cb * 256);
      v4u g0, g1;
#pragma unroll 1
      for (unsigned spins = 0; spins < SPIN_CAP; ++spins) {
        g0 = ld128(rw, l16, sgb);
        g1 = ld128(rw, l16, sgb + 1024);
        asm volatile("" ::: "memory");
        if (__all(g0.y == tag32 && g0.w == tag32 && g1.y == tag32 && g1.w == tag32)) break;
      }
      const float t1 = rowsum4(u2f(g0.x) + u2f(g1.x));
      const float t2 = rowsum4(u2f(g0.z) + u2f(g1.z));
      const float mu = t1 * (1.f / D_);
      const float r = __builtin_amdgcn_rsqf(fmaxf(t2 * (1.f / D_) - mu * mu, 0.f) + EPS);
      STAMP(17);
      gate_publish(xv, mu, r, WS_X3, WS_ST3);
      STAMP(18);
      publish(flags + (4 * 256 + P * 16 + m) * 16, tag, tid);
      STAMP(19);
    } else {
      LAS unsigned char* tb_ = L + L_TR + w * 2048;
#pragma unroll
      for (int e = 0; e < 2; ++e)
        *(LAS v4u*)(tb_ + s * 128 + 16 * ((2 * q4 + e) ^ (s & 7))) = v4u{pk(xv[8 * e], xv[8 * e + 1]), pk(xv[8 * e + 2], xv[8 * e + 3]), pk(xv[8 * e + 4], xv[8 * e + 5]), pk(xv[8 * e + 6], xv[8 * e + 7])};
      const int s8 = lane >> 3;
      GAS unsigned char* o = (GAS unsigned char*)p.out + (64 * tb + 32 * (s8 >> 2) + 8 * w + 2 * (s8 & 3)) * 1024 + 128 * cb + 16 * (lane & 7);
#pragma unroll
      for (int e = 0; e < 2; ++e) *(GAS v4u*)(o + 1024 * e) = *(LAS v4u*)(tb_ + 1024 * e + s8 * 128 + 16 * ((lane & 7) ^ s8));
    }
  }
  if (b == 0 && tid == 0) __hip_atomic_store((gu64*)(p.ws + WS_GEN), tag, RLX_AGENT);
  STAMP(25);
  asm volatile(".Lh3_code_end:");
}

extern "C" void kernel_launch(void* const* d_in, const int* in_sizes, int n_in, void* d_out, int out_size, void* d_ws, size_t ws_size,
                              hipStream_t stream) {
  Params p;
  p.q = (const unsigned short*)d_in[0]; p.k = (const unsigned short*)d_in[1]; p.v = (const unsigned short*)d_in[2];
  p.sk = (const unsigned short*)d_in[3]; p.hk = (const unsigned short*)d_in[4]; p.hD = (const unsigned short*)d_in[5];
  p.w1 = (const unsigned short*)d_in[7]; p.b1 = (const unsigned short*)d_in[8]; p.w2 = (const unsigned short*)d_in[9];
  p.b2 = (const unsigned short*)d_in[10];
  p.out = (unsigned short*)d_out; p.ws = (unsigned char*)d_ws;
  (void)hipFuncSetAttribute((const void*)h3_mixer_kernel, hipFuncAttributeMaxDynamicSharedMemorySize, LDS_BYTES);
  hipLaunchKernelGGL(h3_mixer_kernel, dim3(256), dim3(256), LDS_BYTES, stream, p);
}
```

```cpp
#include <hip/hip_runtime.h>
#include <hip/hip_bf16.h>
#include <stdint.h>
#include <type_traits>

#define GAS __attribute__((address_space(1)))
#define LAS __attribute__((address_space(3)))
#define CAS __attribute__((address_space(4)))
typedef unsigned v4u __attribute__((ext_vector_type(4)));
typedef unsigned v2u __attribute__((ext_vector_type(2)));
typedef float f32x2 __attribute__((ext_vector_type(2)));
typedef float f32x4 __attribute__((ext_vector_type(4)));
typedef __bf16 bf16x2 __attribute__((ext_vector_type(2)));
typedef short s16x4 __attribute__((ext_vector_type(4)));
typedef __amdgpu_buffer_rsrc_t rsrc_t;
#define SC1 16
#define SCHED() __builtin_amdgcn_sched_barrier(0)

namespace {
constexpr int S_ = 2048, D_ = 512, F_ = 1792;
constexpr int WS_GEN = 0;
constexpr int WS_FLAG = 4096;
constexpr int FLAG_ARR = 256 * 128;
enum { F_U, F_U2, F_Y, F_W2, F_HA0, N_FLAG = F_HA0 + 4 };
constexpr int REC = 128 * 8 * 256;
constexpr int WS_ST1 = WS_FLAG + N_FLAG * FLAG_ARR, WS_SG = WS_ST1 + REC, WS_ST3 = WS_SG + REC;
constexpr int WS_QS = WS_ST3 + REC;
constexpr int WS_U = 2 << 20;
constexpr int WS_YT = WS_U + (2 << 20);
constexpr int XT = 16 * 1024, HT = 56 * 1024;
constexpr int WS_X1 = WS_YT + (2 << 20);
constexpr int WS_X3 = WS_X1 + 128 * XT;
constexpr int WS_H1 = WS_X3 + 128 * XT;
constexpr int WS_H2 = WS_H1 + 128 * HT;
constexpr int WS_WF2 = WS_H2 + 128 * HT;
constexpr int WS_END = WS_WF2 + 8 * 56 * 4096;
static_assert(WS_QS + 2048 * 8 <= WS_U, "ws map");
constexpr int LDS_BYTES = 163840;
constexpr int L_W1 = LDS_BYTES - 114688;
constexpr int L_H = 0;
constexpr int H_CS = 4416, H_CH = 4 * H_CS;
constexpr int L_U = 35328;
constexpr int U_CS = 5120;
constexpr int L_UT = L_U;
constexpr int L_WR = L_W1 + 13 * 7168;
constexpr int L_CX = 0;
constexpr int L_YT = 0;
constexpr int L_SIG = 55808;
constexpr int L_TAB = 10240;
constexpr float EPS = 1e-5f;
constexpr int DLY_BG = 10, PRE_F0 = 20, PRE_F1 = 2, POLL_SLP = 2;
constexpr int W1EARLY = 2, FWI = 4, W2A = 2, W2Y = 3, W2PRE = 6;
constexpr unsigned SPIN_CAP = 1u << 16;

struct Params {
  const unsigned short *q, *k, *v, *sk, *hk, *hD, *w1, *b1, *w2, *b2;
  unsigned short* out;
  unsigned char* ws;
};

template <int B, int E, class Fn> __device__ __forceinline__ void sfor(Fn&& f) {
  if constexpr (B < E) { f(std::integral_constant<int, B>{}); sfor<B + 1, E>(f); }
}
__device__ __forceinline__ float u2f(unsigned x) { return __builtin_bit_cast(float, x); }
__device__ __forceinline__ unsigned f2u(float x) { return __builtin_bit_cast(unsigned, x); }
__device__ __forceinline__ float blo(unsigned x) { return __builtin_bit_cast(float, x << 16); }
__device__ __forceinline__ float bhi(unsigned x) { return __builtin_bit_cast(float, x & 0xffff0000u); }
__device__ __forceinline__ unsigned pk(float a, float b) { bf16x2 r = {(__bf16)a, (__bf16)b}; return __builtin_bit_cast(unsigned, r); }
__device__ __forceinline__ void MF16(f32x4& acc, const v4u& a, const v4u& b) { asm volatile("v_mfma_f32_16x16x32_bf16 %0, %1, %2, %0" : "+v"(acc) : "v"(a), "v"(b)); }
__device__ __forceinline__ void MF16Z(f32x4& acc, const v4u& a, const v4u& b) { asm volatile("v_mfma_f32_16x16x32_bf16 %0, %1, %2, 0" : "=&v"(acc) : "v"(a), "v"(b)); }
__device__ __forceinline__ void MF16A(f32x4& acc, const v4u& a, const v4u& b) { asm volatile("v_mfma_f32_16x16x32_bf16 %0, %1, %2, %0" : "+v"(acc) : "a"(a), "v"(b)); }
__device__ __forceinline__ void MF16A0(f32x4& acc, const v4u& a, const v4u& b) { asm volatile("v_mfma_f32_16x16x32_bf16 %0, %1, %2, 0" : "=&v"(acc) : "a"(a), "v"(b)); }
#define NOPS(n) do { SCHED(); asm volatile("s_nop " #n); SCHED(); } while (0)
__device__ __forceinline__ void acc2(unsigned u, float& s1, float& s2) {
  const bf16x2 v = __builtin_bit_cast(bf16x2, u), one = __builtin_bit_cast(bf16x2, 0x3f803f80u);
  s1 = __builtin_amdgcn_fdot2_f32_bf16(v, one, s1, false);
  s2 = __builtin_amdgcn_fdot2_f32_bf16(v, v, s2, false);
}
__device__ __forceinline__ rsrc_t mk(const void* p, int bytes) { return __builtin_amdgcn_make_buffer_rsrc((void*)p, 0, bytes, 0x00020000); }
__device__ __forceinline__ v4u ld128(rsrc_t r, int voff, int soff) { return __builtin_amdgcn_raw_buffer_load_b128(r, voff, soff, SC1); }
__device__ __forceinline__ void st128(v4u v, rsrc_t r, int voff, int soff) {
  asm volatile("" : "+v"(v));
  __builtin_amdgcn_raw_buffer_store_b128(v, r, voff, soff, SC1);
  asm volatile("s_nop 1" :: "v"(v) : "memory");
}
__device__ __forceinline__ float sum16(float x) {
  x += u2f(__builtin_amdgcn_update_dpp(0u, f2u(x), 0xB1, 0xf, 0xf, true));
  x += u2f(__builtin_amdgcn_update_dpp(0u, f2u(x), 0x4E, 0xf, 0xf, true));
  x += u2f(__builtin_amdgcn_update_dpp(0u, f2u(x), 0x141, 0xf, 0xf, true));
  x += u2f(__builtin_amdgcn_update_dpp(0u, f2u(x), 0x140, 0xf, 0xf, true));
  return x;
}
#define DRAIN() asm volatile("s_waitcnt vmcnt(0)" ::: "memory")
#define BAR() do { asm volatile("s_waitcnt lgkmcnt(0)" ::: "memory"); __builtin_amdgcn_s_barrier(); asm volatile("" ::: "memory"); } while (0)

__device__ __forceinline__ void nop0() { asm volatile("s_nop 0"); }
__device__ __forceinline__ void setm0(unsigned x) { asm volatile("s_mov_b32 m0, %0" :: "s"(x) : "memory"); }
template <int IMM> __device__ __forceinline__ void dma16i(rsrc_t r, int voff, int soff) {
  asm volatile("buffer_load_dwordx4 %0, %1, %2 offen offset:%3 lds" :: "v"(voff), "s"(r), "s"(soff), "i"(IMM) : "memory");
}
template <int IMM> __device__ __forceinline__ void ldc128(v4u& d, rsrc_t r, int voff, int soff) {
  asm volatile("buffer_load_dwordx4 %0, %1, %2, %3 offen offset:%4 sc1" : "=v"(d) : "v"(voff), "s"(r), "s"(soff), "i"(IMM));
}
template <int IMM> __device__ __forceinline__ void ldv128(v4u& d, rsrc_t r, int voff) {
  asm volatile("buffer_load_dwordx4 %0, %1, %2, 0 offen offset:%3" : "=v"(d) : "v"(voff), "s"(r), "i"(IMM));
}
__device__ __forceinline__ void pin4(v4u& x) { asm volatile("" : "+v"(x)); }
__device__ __forceinline__ void pinf4(f32x4& x) { asm volatile("" : "+v"(x)); }
__device__ __forceinline__ void pini(int& x) { asm volatile("" : "+v"(x)); }
__device__ __forceinline__ void pinf(float& x) { asm volatile("" : "+v"(x)); }
__device__ __forceinline__ void dma16(rsrc_t r, int voff, int soff, unsigned lds_dst) {
  asm volatile("s_mov_b32 m0, %2\n\ts_nop 0\n\tbuffer_load_dwordx4 %0, %1, %3 offen lds" :: "v"(voff), "s"(r), "s"(lds_dst), "s"(soff) : "memory");
}
#define LDV32(d, r, voff) asm volatile("buffer_load_dword %0, %1, %2, 0 offen" : "=v"(d) : "v"(voff), "s"(r))
#define LDV16(d, r, voff) asm volatile("buffer_load_ushort %0, %1, %2, 0 offen" : "=v"(d) : "v"(voff), "s"(r))
#define LDV128(d, r, voff, imm) asm volatile("buffer_load_dwordx4 %0, %1, %2, 0 offen offset:%3" : "=v"(d) : "v"(voff), "s"(r), "i"(imm))
#define LDA128(d, r, voff, soff, imm) asm volatile("buffer_load_dwordx4 %0, %1, %2, %3 offen offset:%4 sc1" : "=a"(d) : "v"(voff), "s"(r), "s"(soff), "i"(imm))
template <int N> __device__ __forceinline__ void waitv() { asm volatile("s_waitcnt vmcnt(%0)" :: "i"(N) : "memory"); }
#define WAITV(n) waitv<n>()
#define PIN(x) asm volatile("" : "+v"(x))
#define PINS(x) asm volatile("" : "+s"(x))
__device__ __forceinline__ v2u trd(LAS unsigned char* a) { return __builtin_bit_cast(v2u, __builtin_amdgcn_ds_read_tr16_b64_v4i16((LAS s16x4*)a)); }
__device__ __forceinline__ int usw(int y) { return ((y >> 1) & 3) << 1; }
__device__ __forceinline__ bool all_eq(v2u x, unsigned long long tag) { return __all(x.x == (unsigned)tag && x.y == (unsigned)(tag >> 32)); }
__device__ __forceinline__ void poll(rsrc_t r, int voff, int soff, unsigned long long tag) {
  bool miss = true;
#pragma unroll 1
  for (unsigned spins = 0; spins < SPIN_CAP; ++spins) {
    if (miss) {
      const v2u x = __builtin_amdgcn_raw_buffer_load_b64(r, voff, soff, SC1);
      miss = x.x != (unsigned)tag || x.y != (unsigned)(tag >> 32);
    }
    asm volatile("" ::: "memory");
    if (!__builtin_amdgcn_ballot_w64(miss)) break;
    __builtin_amdgcn_s_sleep(POLL_SLP);
  }
  __builtin_amdgcn_fence(__ATOMIC_ACQUIRE, "wavefront");
}
__device__ __forceinline__ void set_flag(rsrc_t r, int soff, unsigned long long tag, int lane) {
  if (lane == 0) __builtin_amdgcn_raw_buffer_store_b64(v2u{(unsigned)tag, (unsigned)(tag >> 32)}, r, 0, soff, SC1);
}
template <int N> __device__ __forceinline__ bool rec_ok(const v4u (&g)[N], unsigned tag) {
  bool ok = true;
#pragma unroll
  for (int i = 0; i < N; ++i) ok &= g[i].y == tag && g[i].w == tag;
  return __all(ok);
}
template <int N> __device__ __forceinline__ void gather_fix(v4u (&g)[N], rsrc_t r, int l16, int soff, unsigned tag) {
  if (__builtin_expect(!rec_ok(g, tag), 0)) {
#pragma unroll 1
    for (unsigned spins = 0; spins < SPIN_CAP; ++spins) {
#pragma unroll
      for (int i = 0; i < N; ++i) g[i] = ld128(r, l16 + 1024 * i, soff);
      asm volatile("" ::: "memory");
      if (rec_ok(g, tag)) break;
    }
  }
  __builtin_amdgcn_fence(__ATOMIC_ACQUIRE, "wavefront");
}
__device__ __forceinline__ void swap16(float& a, float& c) { asm("s_nop 1\n\tv_permlane16_swap_b32 %0, %1" : "+v"(a), "+v"(c)); }
__device__ __forceinline__ void swap32(float& a, float& c) { asm("s_nop 1\n\tv_permlane32_swap_b32 %0, %1" : "+v"(a), "+v"(c)); }
__device__ __forceinline__ void ln2_consts(const v4u (&g)[4], int q4, f32x2 (&gc)[2], f32x2 (&gs)[2], unsigned (&mh)[2], unsigned (&sh)[2]) {
  float a0 = u2f(g[0].x) + u2f(g[1].x), a1 = u2f(g[2].x) + u2f(g[3].x), b0 = u2f(g[0].z) + u2f(g[1].z), b1 = u2f(g[2].z) + u2f(g[3].z);
  swap16(a0, a1); swap16(b0, b1);
  a0 += a1; b0 += b1; a1 = a0; b1 = b0;
  swap32(a0, a1); swap32(b0, b1);
  const float mu = (a0 + a1) * (1.f / D_), var = fmaxf((b0 + b1) * (1.f / D_) - mu * mu, 0.f);
  const float r1 = __builtin_amdgcn_rsqf(var + EPS);
  float r0 = r1 * __builtin_amdgcn_rsqf(var * r1 * r1 + EPS), s0 = __builtin_amdgcn_rcpf(r0);
  const unsigned h = pk(-mu, s0);
  float m0 = u2f(pk(-mu, -mu - blo(h))), t0 = u2f(pk(s0, s0 - bhi(h)));
  float rr = r0, s1 = s0, m1 = m0, t1 = t0;
  swap16(r0, rr); swap16(s0, s1); swap16(m0, m1); swap16(t0, t1);
  const f32x2 r = {r0, rr}, cB = -2.302208198f * r, cA = cB * 0.044715f * r * r;
  gc[0] = f32x2{cA[0], cB[0]}; gc[1] = f32x2{cA[1], cB[1]}; gs[0] = f32x2{s0, s0}; gs[1] = f32x2{s1, s1};
  mh[0] = q4 ? 0u : f2u(m0); mh[1] = q4 ? 0u : f2u(m1); sh[0] = q4 ? 0u : f2u(t0); sh[1] = q4 ? 0u : f2u(t1);
#pragma unroll
  for (int i = 0; i < 2; ++i) { PIN(mh[i]); PIN(sh[i]); PIN(gc[i]); PIN(gs[i]); }
}
__device__ __forceinline__ void gelu8(f32x4 y0, f32x4 y1, f32x2 c0, f32x2 s0, f32x2 c1, f32x2 s1, unsigned (&o)[4]) {
  const f32x2 a = {y0[0], y0[1]}, b = {y0[2], y0[3]}, c = {y1[0], y1[1]}, d = {y1[2], y1[3]};
  asm volatile(
      "v_pk_mul_f32 v[240:241], %4, %4\n\tv_pk_mul_f32 v[242:243], %5, %5\n\t"
      "v_pk_mul_f32 v[244:245], %6, %6\n\tv_pk_mul_f32 v[246:247], %7, %7\n\t"
      "v_pk_fma_f32 v[240:241], v[240:241], %8, %8 op_sel:[0,0,1] op_sel_hi:[1,0,1]\n\tv_pk_fma_f32 v[242:243], v[242:243], %8, %8 op_sel:[0,0,1] op_sel_hi:[1,0,1]\n\t"
      "v_pk_fma_f32 v[244:245], v[244:245], %10, %10 op_sel:[0,0,1] op_sel_hi:[1,0,1]\n\tv_pk_fma_f32 v[246:247], v[246:247], %10, %10 op_sel:[0,0,1] op_sel_hi:[1,0,1]\n\t"
      "v_pk_mul_f32 v[240:241], v[240:241], %4\n\tv_pk_mul_f32 v[242:243], v[242:243], %5\n\t"
      "v_pk_mul_f32 v[244:245], v[244:245], %6\n\tv_pk_mul_f32 v[246:247], v[246:247], %7\n\t"
      "v_exp_f32 v240, v240\n\tv_exp_f32 v241, v241\n\t"
      "v_exp_f32 v242, v242\n\tv_exp_f32 v243, v243\n\t"
      "v_exp_f32 v244, v244\n\tv_exp_f32 v245, v245\n\t"
      "v_exp_f32 v246, v246\n\tv_exp_f32 v247, v247\n\t"
      "v_pk_fma_f32 v[240:241], v[240:241], %9, %9\n\tv_pk_fma_f32 v[242:243], v[242:243], %9, %9\n\t"
      "v_pk_fma_f32 v[244:245], v[244:245], %11, %11\n\tv_pk_fma_f32 v[246:247], v[246:247], %11, %11\n\t"
      "v_rcp_f32 v240, v240\n\tv_rcp_f32 v241, v241\n\t"
      "v_rcp_f32 v242, v242\n\tv_rcp_f32 v243, v243\n\t"
      "v_rcp_f32 v244, v244\n\tv_rcp_f32 v245, v245\n\t"
      "v_rcp_f32 v246, v246\n\tv_rcp_f32 v247, v247\n\t"
      "v_pk_mul_f32 v[240:241], v[240:241], %4\n\tv_pk_mul_f32 v[242:243], v[242:243], %5\n\t"
      "v_pk_mul_f32 v[244:245], v[244:245], %6\n\tv_pk_mul_f32 v[246:247], v[246:247], %7\n\t"
      "v_cvt_pk_bf16_f32 %0, v240, v241\n\tv_cvt_pk_bf16_f32 %1, v242, v243\n\t"
      "v_cvt_pk_bf16_f32 %2, v244, v245\n\tv_cvt_pk_bf16_f32 %3, v246, v247"
      : "=v"(o[0]), "=v"(o[1]), "=v"(o[2]), "=v"(o[3]) : "v"(a), "v"(b), "v"(c), "v"(d), "v"(c0), "v"(s0), "v"(c1), "v"(s1)
      : "v240", "v241", "v242", "v243", "v244", "v245", "v246", "v247");
}
__device__ __forceinline__ void swap_rows(unsigned (&o)[4]) {
  asm("s_nop 1\n\tv_permlane16_swap_b32 %0, %2\n\tv_permlane16_swap_b32 %1, %3" : "+v"(o[0]), "+v"(o[1]), "+v"(o[2]), "+v"(o[3]));
}
__device__ __forceinline__ void row_sums(const v4u (&x)[2], float& s1, float& s2) {
  float a = 0.f, c = 0.f, a2 = 0.f, c2 = 0.f;
#pragma unroll
  for (int i = 0; i < 4; ++i) { acc2(x[0][i], a, c); acc2(x[1][i], a2, c2); }
  a += a2; c += c2;
  swap16(a, c);
  a += c; c = a;
  swap32(a, c);
  s1 = a + c; s2 = s1;
  swap16(s1, s2);
  PIN(s1); PIN(s2);
}
__device__ __forceinline__ void unpack16(const v4u (&x)[2], f32x2 (&o)[8]) {
#pragma unroll
  for (int i = 0; i < 8; ++i) { o[i] = f32x2{blo(x[i >> 2][i & 3]), bhi(x[i >> 2][i & 3])}; PIN(o[i]); }
}
__device__ __forceinline__ void gate_n(v4u& o, const f32x2* x, const f32x2* n, const v4u& y) {
#pragma unroll
  for (int i = 0; i < 4; ++i) { const f32x2 a = n[i] * f32x2{blo(y[i]), bhi(y[i])} + x[i]; o[i] = pk(a[0], a[1]); }
}
__device__ __forceinline__ void gate_y(v4u& o, const f32x2* x, const f32x2* y, float mu, float r) {
#pragma unroll
  for (int i = 0; i < 4; ++i) { const f32x2 a = ((x[i] - mu) * y[i]) * r + x[i]; o[i] = pk(a[0], a[1]); }
}
__device__ __forceinline__ void ln1_consts(const v4u (&g)[2], float& mu, float& r) {
  float a = u2f(g[0].x) + u2f(g[1].x), c = u2f(g[0].z) + u2f(g[1].z);
  swap16(a, c);
  a += c; c = a;
  swap32(a, c);
  a += c; c = a;
  swap16(a, c);
  mu = a * (1.f / D_);
  r = __builtin_amdgcn_rsqf(fmaxf(c * (1.f / D_) - mu * mu, 0.f) + EPS);
}
__device__ __forceinline__ void residual(f32x4 (&a)[4], const v4u (&x)[2], const v4u (&b2)[2]) {
#pragma unroll
  for (int jt = 0; jt < 4; ++jt) {
    const unsigned x0 = x[jt >> 1][2 * (jt & 1)], x1 = x[jt >> 1][2 * (jt & 1) + 1], c0 = b2[jt >> 1][2 * (jt & 1)], c1 = b2[jt >> 1][2 * (jt & 1) + 1];
    a[jt] = f32x4{blo(x0), bhi(x0), blo(x1), bhi(x1)} + f32x4{blo(c0), bhi(c0), blo(c1), bhi(c1)};
    PIN(a[jt]);
  }
}
__device__ __forceinline__ void finish16(const f32x4 (&t)[12], const f32x4 (&a)[4], v4u (&x)[2]) {
#pragma unroll
  for (int jt = 0; jt < 4; ++jt) {
    const f32x4 o = (a[jt] + t[jt]) + (t[4 + jt] + t[8 + jt]);
    x[jt >> 1][2 * (jt & 1)] = pk(o[0], o[1]); x[jt >> 1][2 * (jt & 1) + 1] = pk(o[2], o[3]);
  }
}
__device__ __forceinline__ void dma4(rsrc_t r, int voff, int soff, unsigned lds_dst) {
  asm volatile("s_mov_b32 m0, %3\n\ts_nop 0\n\tbuffer_load_dwordx4 %0, %1, %2 offen lds\n\tbuffer_load_dwordx4 %0, %1, %2 offen offset:1024 lds\n\t"
               "buffer_load_dwordx4 %0, %1, %2 offen offset:2048 lds\n\tbuffer_load_dwordx4 %0, %1, %2 offen offset:3072 lds"
               :: "v"(voff), "s"(r), "s"(soff), "s"(lds_dst) : "memory");
}
__device__ __forceinline__ void ldflag(v2u& d, rsrc_t r, int voff, int soff) { asm volatile("buffer_load_dwordx2 %0, %1, %2, %3 offen sc1" : "=v"(d) : "v"(voff), "s"(r), "s"(soff)); }
__device__ __forceinline__ void pina(v4u& x) { asm volatile("" : "+a"(x)); }
}

__global__ void __launch_bounds__(256, 1) h3_mixer_kernel(Params p) {
  extern __shared__ __attribute__((aligned(16))) unsigned char smem[];
  LAS unsigned char* L = (LAS unsigned char*)smem;
  const unsigned Lb = (unsigned)(uintptr_t)L;
  const int tid = threadIdx.x, lane = tid & 63, w = __builtin_amdgcn_readfirstlane(tid >> 6);
  const int b = blockIdx.x;
  const int P = (b & 7) + 8 * (b >> 7), m = (b >> 3) & 15;
  const int th = m >> 3, cb = m & 7, tb = 2 * P + th;
  const int cp = 32 * cb + tb;
  const int s = lane & 15, q4 = lane >> 4, l16 = lane * 16, s8 = lane >> 3, c8 = lane & 7;
  const rsrc_t rw = mk(p.ws, WS_END);
  v2u gen;
  asm volatile("buffer_load_dwordx2 %0, off, %1, 0 sc1" : "=v"(gen) : "s"(rw));

  unsigned ub1[2], touch[3];
  int utr0, utr1, uso, ust, cvA, cvB[4], cvU[2], cvW, cvQ, cvT0, cvT1, cvY;
  float dD;
  int wo[7], wso[8], wvo[2], m224 = 224 * m;
  PINS(m224);
  const rsrc_t r1 = mk(p.w1, D_ * F_ * 2);
  auto w1step = [&](int ks, unsigned dst) {
    const int so = (64 * (ks >> 1) + 8 * (ks & 1)) * (F_ * 2) + m224;
    sfor<0, 7>([&](auto ic) { constexpr int i = ic; if constexpr ((i & 3) == 0) { setm0(dst + i * 1024); nop0(); } dma16i<(i & 3) * 1024>(r1, wo[i], so); });
  };
  auto conv_addr = [&]() {
    const unsigned uD = *(const CAS unsigned*)(p.hD + 2 * cp);
    dD = (w >> 1) ? bhi(uD) : blo(uD);
    cvA = L_H + (w >> 1) * H_CH + (s & 3) * H_CS + 16 * ((s >> 2) + q4) + (w & 1) * 512;
    {
      const int ub = L_U + ((w >> 1) * 2 + (s >> 3)) * U_CS;
#pragma unroll
      for (int i = 0; i < 4; ++i) { const int yy = 7 - (s & 7) + 4 * (w & 1) + i; cvB[i] = ub + yy * 128 + 16 * (q4 ^ usw(yy)); pini(cvB[i]); }
      const int yr = 31 - 16 * (w & 1) - (s & 7);
#pragma unroll
      for (int i = 0; i < 2; ++i) { cvU[i] = ub + (yr - 8) * 128 + 16 * ((7 - 4 * i - q4) ^ usw(yr)); pini(cvU[i]); }
    }
    cvW = L_WR + 7168 + (w >> 1) * 4096 + (lane >> 2) * 128 + 32 * (lane & 3) + 16 * (w & 1);
    cvQ = (64 * tb + 32 * (s8 >> 2) + 8 * w + 2 * (s8 & 3)) * 1024 + 128 * cb + 16 * c8;
    cvT0 = L_CX + 16384 + w * 2048 + (s & 7) * 128 + 16 * ((2 * q4 + (s >> 3)) ^ (s & 7)); cvT1 = L_CX + 16384 + w * 2048 + s8 * 128 + 16 * (c8 ^ s8); cvY = s8 * 65536 + c8 * 16;
    pinf(dD); pini(cvA); pini(cvW); pini(cvQ); pini(cvT0); pini(cvT1); pini(cvY);
  };
  {
    const rsrc_t rk = mk(p.k, S_ * D_ * 2), rv = mk(p.v, S_ * D_ * 2), rs = mk(p.sk, D_ * 8), rb1 = mk(p.b1, F_ * 2), rb2 = mk(p.b2, D_ * 2);
    const int g = 2 * w + (lane >> 5), cpl = lane & 31;
    const int t0 = 64 * tb + 8 * g, ch = 64 * cb + 2 * cpl;
    unsigned kr[11], vr[8];
    v4u sk;
#pragma unroll
    for (int i = 0; i < 11; ++i) { const int o = (t0 - 3 + i) * 1024 + ch * 2; LDV32(kr[i], rk, o); if (i >= 3) LDV32(vr[i - 3], rv, o); }
    LDV128(sk, rs, ch * 8, 0);
    {
      const GAS unsigned char* code0;
      int ncode;
      asm volatile("s_getpc_b64 %0\n.Lh3_code_begin:\n\ts_mov_b32 %1, .Lh3_code_end-.Lh3_code_begin" : "=s"(code0), "=s"(ncode));
      const int ln = (s + 16 * w) * 1024;
      const GAS unsigned char* a = code0 + ((b >> 3) & 7) * 128 + (ln < ncode - 1024 ? ln : 0);
      asm volatile("global_load_dword %0, %1, off" : "=v"(touch[0]) : "v"(a));
      LDV32(touch[1], rw, WS_FLAG + (s < N_FLAG ? s : 0) * FLAG_ARR + (s >= F_HA0 && s < N_FLAG ? P * 16 + m : cp) * 128);
      LDV16(ub1[0], rb1, (112 * m + 32 * w + s) * 2);
      LDV16(ub1[1], rb1, (112 * m + 32 * w + 16 + s) * 2);
      LDV32(touch[2], rb2, 128 * cb);
    }
#pragma unroll
    for (int i = 0; i < 7; ++i) {
      const int Lc = i * 64 + lane, rho = (Lc * 4682) >> 16, cc = Lc - rho * 14;
      wo[i] = (16 * ((rho >> 2) & 3) + 4 * (rho >> 4) + (rho & 3)) * (F_ * 2) + 16 * cc - (i & 3) * 1024;
      PIN(wo[i]);
    }
    utr0 = L_UT + (32 * (w & 1) + s8) * 128 + 16 * (c8 ^ (s8 >> 1)); utr1 = utr0 ^ 64; uso = s8 * 4096 + c8 * 16;
    ust = L_U + (w & 1) * 2 * U_CS + s8 * 128 + 16 * (c8 ^ usw(s8));
    PIN(utr0); PIN(utr1); PIN(uso); PIN(ust);
    int uta = L_UT + 2 * cpl * 128 + 16 * ((7 - g) ^ (cpl & 7));
#pragma unroll
    for (int e = 0; e < 2; ++e) {
      const int k2 = 2 * tb + e, pr = (k2 * 37) >> 8, j = k2 - 7 * pr;
      const int f0 = 224 * pr + (j > 3 ? 32 * j - 16 : 32 * j), dh = (j == 3 ? 4 : 16), dk = (j == 3 ? 112 : 8);
      wvo[e] = ((j == 3 ? 8 : 4) * (lane >> 5) + (s8 & 3)) * (D_ * 2) + 128 * cb + 16 * c8;
      PIN(wvo[e]);
#pragma unroll
      for (int i = 0; i < 4; ++i) { wso[4 * e + i] = (f0 + dh * (i >> 1) + dk * (i & 1)) * (D_ * 2); PINS(wso[4 * e + i]); }
    }
    PIN(uta);
    asm volatile("s_waitcnt vmcnt(5)" : "+v"(gen), "+v"(kr[0]), "+v"(kr[1]), "+v"(kr[2]), "+v"(kr[3]), "+v"(kr[4]), "+v"(kr[5]), "+v"(kr[6]), "+v"(kr[7]), "+v"(kr[8]),
                 "+v"(kr[9]), "+v"(kr[10]), "+v"(vr[0]), "+v"(vr[1]), "+v"(vr[2]), "+v"(vr[3]), "+v"(vr[4]), "+v"(vr[5]), "+v"(vr[6]), "+v"(vr[7]), "+v"(sk));
    if (w == 1) {
      w1step(P, Lb + L_WR);
    } else if (w == 2) {
      const rsrc_t r2 = mk(p.w2, tb < 28 ? F_ * D_ * 2 : 0);
#pragma unroll
      for (int i = 0; i < 8; ++i) dma16(r2, wvo[i >> 2], wso[i], Lb + L_WR + 7168 + i * 1024);
    }
    const f32x2 s0 = {blo(sk.x), blo(sk.z)}, s1 = {bhi(sk.x), bhi(sk.z)}, s2 = {blo(sk.y), blo(sk.w)}, s3 = {bhi(sk.y), bhi(sk.w)};
    f32x2 kf[11], uu[8];
#pragma unroll
    for (int i = 0; i < 11; ++i) kf[i] = f32x2{blo(kr[i]), bhi(kr[i])};
#pragma unroll
    for (int i = 0; i < 8; ++i) {
      f32x2 a = kf[i] * s3;
      a = kf[i + 1] * s2 + a;
      a = kf[i + 2] * s1 + a;
      a = kf[i + 3] * s0 + a;
      uu[i] = a * f32x2{blo(vr[i]), bhi(vr[i])};
    }
#pragma unroll
    for (int c = 0; c < 2; ++c)
      *(LAS v4u*)(L + uta + c * 128) = v4u{pk(uu[7][c], uu[6][c]), pk(uu[5][c], uu[4][c]), pk(uu[3][c], uu[2][c]), pk(uu[1][c], uu[0][c])};
  }
  const unsigned long long tag = (unsigned)__builtin_amdgcn_readfirstlane(gen.x) + 1ull + ((unsigned long long)(unsigned)__builtin_amdgcn_readfirstlane(gen.y) << 32);
  const unsigned tag32 = (unsigned)tag;
  if (tid == 0) *(LAS v2u*)(L + L_SIG) = v2u{0u, 0u};
  BAR();
  {
    if (w == 1 || w == 2) {
      const rsrc_t rh = mk(p.hk, D_ * S_ * 2), rq_ = mk(p.q, S_ * D_ * 2);
      __builtin_amdgcn_s_sleep(DLY_BG);
#pragma unroll
      for (int c = 0; c < 4; ++c) dma4(rh, l16 + 4 * c - 12, (2 * cp + w - 1) * 4096, Lb + L_H + (w - 1) * H_CH + c * H_CS + 112);
      v4u qrow[4];
      const int vo = (64 * tb + 8 * cb + 4 * (w - 1) + q4) * 1024 + 16 * s;
      sfor<0, 4>([&](auto ec) { constexpr int e = ec; ldv128<256 * e>(qrow[e], rq_, vo); });
      conv_addr();
      asm volatile("s_waitcnt vmcnt(0)" : "+v"(qrow[0]), "+v"(qrow[1]), "+v"(qrow[2]), "+v"(qrow[3]) :: "memory");
      const v4u h0 = *(const CAS v4u*)(p.hk + (2 * cp + w - 1) * S_);
      v4u o = {0u, q4 == 2 ? h0.x : 0u, q4 == 2 ? h0.y : (q4 == 1 ? h0.x : 0u), q4 == 2 ? h0.z : (q4 == 1 ? h0.y : h0.x)};
      if (c8 != 7) o = v4u{0u, 0u, 0u, 0u};
      if (s < 8 && lane != 55) *(LAS v4u*)(L + L_H + (w - 1) * H_CH + q4 * H_CS + c8 * 16) = o;
      float s1 = 0.f, s2 = 0.f;
#pragma unroll
      for (int e = 0; e < 4; ++e)
#pragma unroll
        for (int i = 0; i < 4; ++i) acc2(qrow[e][i], s1, s2);
      s1 = sum16(s1); s2 = sum16(s2);
      const float mu = s1 * (1.f / D_), rs = __builtin_amdgcn_rsqf(fmaxf(s2 * (1.f / D_) - mu * mu, 0.f) + EPS);
      if (s == 0) __builtin_amdgcn_raw_buffer_store_b64(v2u{f2u(mu), f2u(rs)}, rw, q4 * 8, WS_QS + (64 * tb + 8 * cb + 4 * (w - 1)) * 8, SC1);
#pragma unroll 1
      for (unsigned spins = 0; spins < 64 * SPIN_CAP; ++spins) {
        const v2u sg = *(volatile LAS v2u*)(L + L_SIG);
        if (__builtin_amdgcn_readfirstlane(sg.x & sg.y)) break;
        __builtin_amdgcn_s_sleep(1);
      }
#pragma unroll
      for (int i = 0; i < W1EARLY; ++i) w1step(w + 2 * i, Lb + L_W1 + (w + 2 * i) * 7168);
    } else {
      const int chl = w & 1;
      v4u t[4];
#pragma unroll
      for (int i = 0; i < 4; ++i) t[i] = *(LAS v4u*)(L + ((i & 1) ? utr1 : utr0) + i * 1024);
#pragma unroll
      for (int i = 0; i < 4; ++i) st128(t[i], rw, uso, WS_U + (64 * cb + 32 * chl + 8 * i) * 4096 + (31 - tb) * 128);
      if (chl) st128(v4u{0u, ~tag32, 0u, ~tag32}, rw, q4 * 2048 + s * 16, WS_ST1 + (tb * 32 + cb) * 256);
      conv_addr();
      DRAIN();
      set_flag(rw, WS_FLAG + (F_U + chl) * FLAG_ARR + cp * 128, tag, lane);
      __builtin_amdgcn_s_sleep(PRE_F0);
      poll(rw, (lane >> 5) * FLAG_ARR + (lane & 31) * 128, WS_FLAG + F_U * FLAG_ARR + cb * 32 * 128, tag);
      v4u ut[4];
#pragma unroll
      for (int i = 0; i < 4; ++i) ut[i] = ld128(rw, l16 + i * 1024, WS_U + (2 * cp + chl) * 4096);
      SCHED();
      *(volatile LAS unsigned*)(L + L_SIG + 4 * chl) = 1u;
#pragma unroll
      for (int i = 0; i < 2; ++i) *(LAS v4u*)(L + L_U + (2 * chl + i) * U_CS + 4096 + l16) = v4u{0u, 0u, 0u, 0u};
#pragma unroll
      for (int i = 0; i < 4; ++i) {
        const unsigned last = i < 3 ? __builtin_amdgcn_readlane(ut[(i + 1) & 3].x, 0) : 0u;
        const unsigned nx = __builtin_amdgcn_update_dpp(last, ut[i].x, 0x130, 0xf, 0xf, false);
        LAS unsigned char* d = L + ust + i * 1024;
        *(LAS v4u*)(d + U_CS) = ut[i];
        *(LAS v4u*)(d) = v4u{__builtin_amdgcn_alignbit(ut[i].y, ut[i].x, 16), __builtin_amdgcn_alignbit(ut[i].z, ut[i].y, 16),
                             __builtin_amdgcn_alignbit(ut[i].w, ut[i].z, 16), __builtin_amdgcn_alignbit(nx, ut[i].w, 16)};
      }
    }
  }
  v2u fw;
  v4u xr[2];
  {
    const int chl = w >> 1, kh = w & 1;
    asm volatile("" : "+v"(touch[0]), "+v"(touch[1]), "+v"(ub1[0]), "+v"(ub1[1]), "+v"(touch[2]));
    BAR();
    constexpr int K0 = 2 * W1EARLY + 1, NST = (16 - K0 + 3) / 4, NPC = 7 * NST;
    int w1s[NST];
    unsigned w1d[NST];
#pragma unroll
    for (int j = 0; j < NST; ++j) { const int ks = K0 + w + 4 * j < 15 ? K0 + w + 4 * j : 15; w1s[j] = (64 * (ks >> 1) + 8 * (ks & 1)) * (F_ * 2) + m224; w1d[j] = Lb + L_W1 + ks * 7168; }
    f32x4 acc[4][2];
    v4u A[3][4], B[2][4], uc[4];
#pragma unroll
    for (int c = 0; c < 4; ++c) { B[0][c] = *(LAS v4u*)(L + cvB[0] + (3 - c) * 1024); A[0][c] = *(LAS v4u*)(L + cvA + 1024 * c); A[1][c] = *(LAS v4u*)(L + cvA + 1024 * c + 64); SCHED(); }
    v4u wf[2];
#pragma unroll
    for (int e = 0; e < 2; ++e) { const v2u lo = trd(L + cvW + 8 * e), hi = trd(L + cvW + 2048 + 8 * e); wf[e] = v4u{lo.x, lo.y, hi.x, hi.y}; }
    const rsrc_t rf = mk(p.ws + WS_WF2, tb < 28 ? 8 * 56 * 4096 : 0), rq_ = mk(p.q, S_ * D_ * 2);
    SCHED();
    sfor<0, 8>([&](auto iic) {
      constexpr int ii = iic;
      LAS unsigned char* bp = L + (cvB[((ii + 1) >> 1) & 3] ^ (((ii + 1) & 1) * 64));
      if constexpr (ii == FWI) {
        WAITV(NST * (FWI - 1));
        __builtin_amdgcn_s_barrier();
        if (w == 0) set_flag(rw, WS_FLAG + F_W2 * FLAG_ARR + cp * 128, tag, lane);
        pin4(xr[0]); pin4(xr[1]);
      }
      SCHED();
      sfor<0, 20>([&](auto nc) {
        constexpr int n = nc, pr = n >> 1, I = n & 1;
        constexpr int c = pr < 4 ? 0 : (pr < 7 ? 1 : (pr < 9 ? 2 : 3)), J = pr < 4 ? pr : (pr < 7 ? pr - 3 : (pr < 9 ? pr - 5 : 3));
        constexpr int vm = (ii >= 1 && n % (20 / NST) == 5 % (20 / NST) && n / (20 / NST) < NST) ? (ii - 1) * NST + n / (20 / NST) : 99, k4 = vm / 7, pc = vm % 7;
        if constexpr (vm < NPC && (pc & 3) == 0) setm0(w1d[k4] + pc * 1024);
        if constexpr (ii == 0 && c == 0) MF16Z(acc[J][I], A[(ii + I) % 3][c], B[ii & 1][J - c]);
        else MF16(acc[J][I], A[(ii + I) % 3][c], B[ii & 1][J - c]);
        if constexpr (ii + 1 < 8 && (n & 1) && n < 16) {
          constexpr int r = n >> 1;
          SCHED();
          if constexpr (r & 1) A[(ii + 2) % 3][r >> 1] = *(LAS v4u*)(L + cvA + 1024 * (r >> 1) + 64 * (ii + 2));
          else B[(ii + 1) & 1][r >> 1] = *(LAS v4u*)(bp + (3 - (r >> 1)) * 1024);
          SCHED();
        }
        if constexpr (ii == 0 && (n == 4 || n == 8)) ldv128<(n / 8) * 1024>(xr[n / 8], rq_, cvQ);
        if constexpr (ii == 0 && (n == 12 || n == 18)) st128(wf[n / 16], rf, l16 + (n / 16) * 1024, ((cb * 56 + 2 * tb) * 4 + 2 * w) * 1024);
        if constexpr (ii == 7 && (n & 1) && n < 8) { SCHED(); uc[n >> 1] = *(LAS v4u*)(L + cvU[(n >> 1) & 1] + (n < 4 ? 1024 : 0)); SCHED(); }
        if constexpr (vm < NPC) dma16i<(pc & 3) * 1024>(r1, wo[pc], w1s[k4]);
      });
      SCHED();
    });
    NOPS(7);
    ldflag(fw, rw, (lane & 31) * 128, WS_FLAG + F_W2 * FLAG_ARR + cb * 32 * 128);
    LAS f32x4* cx = (LAS f32x4*)(L + L_CX);
    f32x4 mine[4];
    BAR();
    if (kh) {
      asm volatile("");
#pragma unroll
      for (int g = 0; g < 4; ++g) { cx[(w * 4 + g) * 64 + lane] = acc[g >> 1][g & 1]; mine[g] = acc[2 + (g >> 1)][g & 1]; }
    } else {
      asm volatile("");
#pragma unroll
      for (int g = 0; g < 4; ++g) { cx[(w * 4 + g) * 64 + lane] = acc[2 + (g >> 1)][g & 1]; mine[g] = acc[g >> 1][g & 1]; }
    }
#pragma unroll
    for (int g = 0; g < 4; ++g) {
      mine[g] += f32x4{blo(uc[g][3]), blo(uc[g][2]), blo(uc[g][1]), blo(uc[g][0])} * dD;
      pinf4(mine[g]);
    }
    BAR();
    f32x4 ot[4];
#pragma unroll
    for (int g = 0; g < 4; ++g) ot[g] = cx[((w ^ 1) * 4 + g) * 64 + lane];
    SCHED();
#pragma unroll
    for (int jj = 0; jj < 2; ++jj) {
      const f32x4 y0 = mine[2 * jj] + ot[2 * jj], y1 = mine[2 * jj + 1] + ot[2 * jj + 1];
      *(LAS v4u*)(L + cvT0 + jj * 1024) = v4u{pk(y0[0], y0[1]), pk(y0[2], y0[3]), pk(y1[0], y1[1]), pk(y1[2], y1[3])};
    }
    v4u yo[2];
#pragma unroll
    for (int jj = 0; jj < 2; ++jj) yo[jj] = *(LAS v4u*)(L + cvT1 + jj * 1024);
#pragma unroll
    for (int jj = 0; jj < 2; ++jj) st128(yo[jj], rw, cvY, WS_YT + ((16 * kh + 8 * jj) * 512 + 2 * cp + chl) * 128);
  }
  {
    LAS unsigned char* t = L + L_CX + 24576 + w * 2048;
    *(LAS v4u*)(t + l16) = xr[0];
    *(LAS v4u*)(t + 1024 + l16) = xr[1];
    xr[0] = *(LAS v4u*)(t + s * 128 + 32 * q4);
    xr[1] = *(LAS v4u*)(t + s * 128 + 32 * q4 + 16);
  }
  f32x2 xq[8];
  unpack16(xr, xq);
  int qso = (64 * tb + 32 * ((s >> 2) & 1) + 8 * w + 2 * (s & 3) + (s >> 3)) * 8;
  int ytr = L_YT + (16 * q4 + (s >> 2)) * 128 + 32 * w + 8 * (s & 3);
  int wa0 = L_W1 + (lane >> 2) * 224 + 8 * (lane & 3), wa1 = wa0 + 57344;
  v4u ones = {0x3f803f80u, 0x3f803f80u, 0x3f803f80u, 0x3f803f80u};
  PIN(qso); PIN(ytr); PIN(wa0); PIN(wa1); PIN(ones);
  DRAIN();
  BAR();
  if (w == 0) set_flag(rw, WS_FLAG + F_Y * FLAG_ARR + cp * 128, tag, lane);
  if (w == 3) w1step(0, Lb + L_W1);
  v4u w2f[14][4];
  int w2s = WS_WF2 + (cb * 56 + 14 * w) * 4096;
  PIN(fw); PINS(w2s);
  if (!all_eq(fw, tag)) poll(rw, (lane & 31) * 128, WS_FLAG + F_W2 * FLAG_ARR + cb * 32 * 128, tag);
#define W2LOAD(k0, k1) _Pragma("unroll") for (int kk = k0; kk < k1; ++kk) _Pragma("unroll") for (int jt = 0; jt < 4; ++jt) LDA128(w2f[kk][jt], rw, l16, w2s + kk * 4096, jt * 1024)
  W2LOAD(0, W2A);
  f32x4 cs[2];
  auto colsum = [&](auto k0, auto k1) {
    sfor<0, 2>([&](auto fc) {
      constexpr int fi = fc;
      v4u a[k1 - k0];
#pragma unroll
      for (int ks = k0; ks < k1; ++ks) {
        LAS unsigned char* ap = L + (ks < 8 ? wa0 : wa1) + (w == 3 ? 6 * 32 : (2 * w + fi) * 32) + (ks & 7) * 7168;
        const v2u lo = trd(ap), hi = trd(ap + 3584);
        a[ks - k0] = v4u{lo.x, lo.y, hi.x, hi.y};
      }
      sfor<0, k1 - k0>([&](auto ic) { constexpr int i = ic; if constexpr (i + k0 == 1) MF16Z(cs[fi], ones, a[i]); else MF16(cs[fi], ones, a[i]); });
    });
  };
  colsum(std::integral_constant<int, 1>{}, std::integral_constant<int, 16>{});
  if (w == 0) {
    __builtin_amdgcn_s_sleep(PRE_F1);
    poll(rw, (lane < 32 ? cb * 32 + lane : c8 * 32 + tb) * 128, WS_FLAG + F_Y * FLAG_ARR, tag);
  }
  BAR();
  v4u yp[2];
  float rs1, rs2;
  {
    v4u yt[2];
    v2u qs;
    ldc128<0>(yt[0], rw, l16, WS_YT + (tb * 512 + 64 * cb) * 128 + w * 2048);
    ldc128<1024>(yt[1], rw, l16, WS_YT + (tb * 512 + 64 * cb) * 128 + w * 2048);
    ldflag(qs, rw, qso, WS_QS);
    W2LOAD(W2A, W2A + W2Y);
    asm volatile("s_waitcnt vmcnt(%3)" : "+v"(yt[0]), "+v"(yt[1]), "+v"(qs) : "i"(4 * W2Y) : "memory");
#pragma unroll
    for (int i = 0; i < 2; ++i) *(LAS v4u*)(L + L_YT + (w * 2 + i) * 1024 + l16) = yt[i];
    SCHED();
    f32x2 xn[8];
#pragma unroll
    for (int i = 0; i < 8; ++i) { xn[i] = (xq[i] - u2f(qs.x)) * u2f(qs.y); PIN(xn[i]); }
    BAR();
#pragma unroll
    for (int e = 0; e < 2; ++e) { const v2u lo = trd(L + ytr + 1024 * e), hi = trd(L + ytr + 1024 * e + 512); yp[e] = v4u{lo.x, lo.y, hi.x, hi.y}; }
#pragma unroll
    for (int e = 0; e < 2; ++e) {
      gate_n(xr[e], xq + 4 * e, xn + 4 * e, yp[e]);
      st128(xr[e], rw, l16 + e * 1024, WS_X1 + (tb * 4 + w) * XT + 2 * cb * 1024);
      SCHED();
    }
  }
  row_sums(xr, rs1, rs2);
  W2LOAD(W2A + W2Y, W2PRE);
  colsum(std::integral_constant<int, 0>{}, std::integral_constant<int, 1>{});
  WAITV(4 * (W2PRE - W2A - W2Y));
  if (q4 == 0) st128(v4u{f2u(rs1), tag32, f2u(rs2), tag32}, rw, l16, WS_ST1 + ((tb * 4 + w) * 8 + cb) * 256);
  NOPS(7);
  pinf4(cs[0]); pinf4(cs[1]);
#pragma unroll
  for (int fi = 0; fi < 2; ++fi) {
    const float c = cs[fi][0];
    const unsigned hi = pk(c, c), lo = pk(c - blo(hi), c - blo(hi));
    if (q4 == 0 && 2 * w + fi < 7) *(LAS v4u*)(L + L_TAB + (2 * w + fi) * 256 + s * 16) = v4u{hi, lo, ub1[fi] | (ub1[fi] << 16), 0u};
  }
  BAR();
  v4u we[7];
#pragma unroll
  for (int ft = 0; ft < 7; ++ft) { we[ft] = *(LAS v4u*)(L + L_TAB + ft * 256 + s * 16); pina(we[ft]); }
  auto invalidate_later = [&]() {
    st128(v4u{0u, ~tag32, 0u, ~tag32}, rw, (q4 & 1) * REC + s * 16, WS_SG + ((tb * 4 + w) * 8 + cb) * 256);
  };

  const int TT = 8 * P + 2 * w, TO = tb * 4 + w;
  int xb0 = WS_X1 + TT * XT, xb1 = WS_X3 + TT * XT, rg0 = WS_ST1 + TT * 2048, rg1 = WS_ST3 + TT * 2048;
  int hp60 = WS_H1 + TT * HT + (7 * (m >> 1) + 3) * 1024 + (m & 1) * 512, hp61 = hp60 + (WS_H2 - WS_H1);
  int hp0 = WS_H1 + TT * HT + 7 * (m >> 1) * 1024 + (m & 1) * 4096, hp1 = hp0 + (WS_H2 - WS_H1);
  int hq0[4], hq1[4], ro[3];
#pragma unroll
  for (int t = 0; t < 4; ++t) { hq0[t] = WS_H1 + (tb * 4 + ((w + 1 + t) & 3)) * HT + 14 * w * 1024; hq1[t] = hq0[t] + (WS_H2 - WS_H1); PINS(hq0[t]); PINS(hq1[t]); }
#pragma unroll
  for (int t = 0; t < 3; ++t) ro[t] = (((w + 1 + t) & 3) * 3 + t) * 4096 + l16;
  int rsg = WS_SG + TO * 2048, rso = rsg + cb * 256, ro3 = WS_ST3 + TO * 2048 + cb * 256, xo3 = WS_X3 + TO * XT + 2 * cb * 1024;
  int flm = WS_FLAG + F_HA0 * FLAG_ARR + (P * 16 + m) * 128, flw = WS_FLAG + F_HA0 * FLAG_ARR + (P * 16 + 4 * w) * 128;
  PINS(xb0); PINS(xb1); PINS(rg0); PINS(rg1); PINS(hp60); PINS(hp61); PINS(hp0); PINS(hp1); PINS(rsg); PINS(rso); PINS(ro3); PINS(xo3); PINS(flm); PINS(flw);
  const int fo = (lane & 3) * 128, h6o = (q4 & 1) * HT + (q4 >> 1) * 256 + s * 16, b2o = 128 * cb + 32 * q4, ri = w * 12288 + l16;
  const int oo = w * 12288 + s * 128 + 16 * ((2 * q4) ^ (s & 7)), oi = w * 12288 + s8 * 128 + 16 * (c8 ^ s8), oso = (32 * (s8 >> 2) + 2 * (s8 & 3)) * 1024 + 16 * c8;
  const rsrc_t rb2 = mk(p.b2, D_ * 2), rout = mk(p.out, S_ * D_ * 2);
  v4u xf[2][16], hf[14][4], ga[4], gs[2], b2p[2];
  v2u al[5], ah[5], fh;
  f32x4 a1[2][7], a2[4][4], rin[12];
  f32x2 gc[2], gd[2], yq[8];
  unsigned mh[2], sh[2], go[4];
  __builtin_amdgcn_s_sleep(10);
  ldc128<0>(ga[0], rw, l16, rg0);
  ldc128<1024>(ga[1], rw, l16, rg0);
  ldc128<2048>(ga[2], rw, l16, rg0);
  ldc128<3072>(ga[3], rw, l16, rg0);
  asm volatile("s_waitcnt vmcnt(0)" : "+v"(ga[0]), "+v"(ga[1]), "+v"(ga[2]), "+v"(ga[3]) :: "memory");
  gather_fix(ga, rw, l16, rg0, tag32);
  ldc128<0>(xf[0][0], rw, l16, xb0 + 0);
  ldc128<0>(xf[1][0], rw, l16, xb0 + XT + 0);
  ldc128<1024>(xf[0][1], rw, l16, xb0 + 0);
  ldc128<1024>(xf[1][1], rw, l16, xb0 + XT + 0);
  ldc128<2048>(xf[0][2], rw, l16, xb0 + 0);
  ldc128<2048>(xf[1][2], rw, l16, xb0 + XT + 0);
  ldc128<3072>(xf[0][3], rw, l16, xb0 + 0);
  ldc128<3072>(xf[1][3], rw, l16, xb0 + XT + 0);
  ldc128<0>(xf[0][4], rw, l16, xb0 + 4096);
  ldc128<0>(xf[1][4], rw, l16, xb0 + XT + 4096);
  LDV128(b2p[0], rb2, b2o, 0);
  LDV128(b2p[1], rb2, b2o, 16);
  invalidate_later();
  al[0] = trd(L + wa0 + 0); ah[0] = trd(L + wa0 + 3584);
  al[1] = trd(L + wa0 + 32); ah[1] = trd(L + wa0 + 3616);
  al[2] = trd(L + wa0 + 64); ah[2] = trd(L + wa0 + 3648);
  al[3] = trd(L + wa0 + 96); ah[3] = trd(L + wa0 + 3680);
  SCHED();
  ln2_consts(ga, q4, gc, gd, mh, sh);
  SCHED();
  al[4] = trd(L + wa0 + 7168); SCHED();
  asm volatile("s_waitcnt vmcnt(12)" :  :: "memory");
  { const v4u a = {al[0].x, al[0].y, ah[0].x, ah[0].y}; MF16Z(a1[0][0], a, xf[0][0]); SCHED();
    ah[4] = trd(L + wa0 + 10752); SCHED();
  asm volatile("s_waitcnt vmcnt(11)" :  :: "memory");
    MF16Z(a1[1][0], a, xf[1][0]); } SCHED();
  al[0] = trd(L + wa0 + 7200); SCHED();
  { const v4u a = {al[1].x, al[1].y, ah[1].x, ah[1].y}; MF16Z(a1[0][1], a, xf[0][0]); SCHED();
    ah[0] = trd(L + wa0 + 10784); SCHED();
    MF16Z(a1[1][1], a, xf[1][0]); } SCHED();
  ldc128<1024>(xf[0][5], rw, l16, xb0 + 4096);
  SCHED();
  al[1] = trd(L + wa0 + 7232); SCHED();
  { const v4u a = {al[2].x, al[2].y, ah[2].x, ah[2].y}; MF16Z(a1[0][2], a, xf[0][0]); SCHED();
    ah[1] = trd(L + wa0 + 10816); SCHED();
    MF16Z(a1[1][2], a, xf[1][0]); } SCHED();
  al[2] = trd(L + wa0 + 7264); SCHED();
  { const v4u a = {al[3].x, al[3].y, ah[3].x, ah[3].y}; MF16Z(a1[0][3], a, xf[0][0]); SCHED();
    ah[2] = trd(L + wa0 + 10848); SCHED();
    MF16Z(a1[1][3], a, xf[1][0]); } SCHED();
  ldc128<1024>(xf[1][5], rw, l16, xb0 + XT + 4096);
  SCHED();
  al[3] = trd(L + wa0 + 14336); SCHED();
  asm volatile("s_waitcnt vmcnt(12)" :  :: "memory");
  { const v4u a = {al[4].x, al[4].y, ah[4].x, ah[4].y}; MF16(a1[0][0], a, xf[0][1]); SCHED();
    ah[3] = trd(L + wa0 + 17920); SCHED();
  asm volatile("s_waitcnt vmcnt(11)" :  :: "memory");
    MF16(a1[1][0], a, xf[1][1]); } SCHED();
  al[4] = trd(L + wa0 + 14368); SCHED();
  { const v4u a = {al[0].x, al[0].y, ah[0].x, ah[0].y}; MF16(a1[0][1], a, xf[0][1]); SCHED();
    ah[4] = trd(L + wa0 + 17952); SCHED();
    MF16(a1[1][1], a, xf[1][1]); } SCHED();
  ldc128<2048>(xf[0][6], rw, l16, xb0 + 4096);
  SCHED();
  al[0] = trd(L + wa0 + 14400); SCHED();
  { const v4u a = {al[1].x, al[1].y, ah[1].x, ah[1].y}; MF16(a1[0][2], a, xf[0][1]); SCHED();
    ah[0] = trd(L + wa0 + 17984); SCHED();
    MF16(a1[1][2], a, xf[1][1]); } SCHED();
  al[1] = trd(L + wa0 + 14432); SCHED();
  { const v4u a = {al[2].x, al[2].y, ah[2].x, ah[2].y}; MF16(a1[0][3], a, xf[0][1]); SCHED();
    ah[1] = trd(L + wa0 + 18016); SCHED();
    MF16(a1[1][3], a, xf[1][1]); } SCHED();
  ldc128<2048>(xf[1][6], rw, l16, xb0 + XT + 4096);
  SCHED();
  al[2] = trd(L + wa0 + 21504); SCHED();
  asm volatile("s_waitcnt vmcnt(12)" :  :: "memory");
  { const v4u a = {al[3].x, al[3].y, ah[3].x, ah[3].y}; MF16(a1[0][0], a, xf[0][2]); SCHED();
    ah[2] = trd(L + wa0 + 25088); SCHED();
  asm volatile("s_waitcnt vmcnt(11)" :  :: "memory");
    MF16(a1[1][0], a, xf[1][2]); } SCHED();
  al[3] = trd(L + wa0 + 21536); SCHED();
  { const v4u a = {al[4].x, al[4].y, ah[4].x, ah[4].y}; MF16(a1[0][1], a, xf[0][2]); SCHED();
    ah[3] = trd(L + wa0 + 25120); SCHED();
    MF16(a1[1][1], a, xf[1][2]); } SCHED();
  ldc128<3072>(xf[0][7], rw, l16, xb0 + 4096);
  SCHED();
  al[4] = trd(L + wa0 + 21568); SCHED();
  { const v4u a = {al[0].x, al[0].y, ah[0].x, ah[0].y}; MF16(a1[0][2], a, xf[0][2]); SCHED();
    ah[4] = trd(L + wa0 + 25152); SCHED();
    MF16(a1[1][2], a, xf[1][2]); } SCHED();
  al[0] = trd(L + wa0 + 21600); SCHED();
  { const v4u a = {al[1].x, al[1].y, ah[1].x, ah[1].y}; MF16(a1[0][3], a, xf[0][2]); SCHED();
    ah[0] = trd(L + wa0 + 25184); SCHED();
    MF16(a1[1][3], a, xf[1][2]); } SCHED();
  ldc128<3072>(xf[1][7], rw, l16, xb0 + XT + 4096);
  SCHED();
  al[1] = trd(L + wa0 + 28672); SCHED();
  asm volatile("s_waitcnt vmcnt(12)" :  :: "memory");
  { const v4u a = {al[2].x, al[2].y, ah[2].x, ah[2].y}; MF16(a1[0][0], a, xf[0][3]); SCHED();
    ah[1] = trd(L + wa0 + 32256); SCHED();
  asm volatile("s_waitcnt vmcnt(11)" :  :: "memory");
    MF16(a1[1][0], a, xf[1][3]); } SCHED();
  al[2] = trd(L + wa0 + 28704); SCHED();
  { const v4u a = {al[3].x, al[3].y, ah[3].x, ah[3].y}; MF16(a1[0][1], a, xf[0][3]); SCHED();
    ah[2] = trd(L + wa0 + 32288); SCHED();
    MF16(a1[1][1], a, xf[1][3]); } SCHED();
  ldc128<0>(xf[0][8], rw, l16, xb0 + 8192);
  SCHED();
  al[3] = trd(L + wa0 + 28736); SCHED();
  { const v4u a = {al[4].x, al[4].y, ah[4].x, ah[4].y}; MF16(a1[0][2], a, xf[0][3]); SCHED();
    ah[3] = trd(L + wa0 + 32320); SCHED();
    MF16(a1[1][2], a, xf[1][3]); } SCHED();
  al[4] = trd(L + wa0 + 28768); SCHED();
  { const v4u a = {al[0].x, al[0].y, ah[0].x, ah[0].y}; MF16(a1[0][3], a, xf[0][3]); SCHED();
    ah[4] = trd(L + wa0 + 32352); SCHED();
    MF16(a1[1][3], a, xf[1][3]); } SCHED();
  ldc128<0>(xf[1][8], rw, l16, xb0 + XT + 8192);
  SCHED();
  al[0] = trd(L + wa0 + 35840); SCHED();
  asm volatile("s_waitcnt vmcnt(12)" :  :: "memory");
  { const v4u a = {al[1].x, al[1].y, ah[1].x, ah[1].y}; MF16(a1[0][0], a, xf[0][4]); SCHED();
    ah[0] = trd(L + wa0 + 39424); SCHED();
  asm volatile("s_waitcnt vmcnt(11)" :  :: "memory");
    MF16(a1[1][0], a, xf[1][4]); } SCHED();
  al[1] = trd(L + wa0 + 35872); SCHED();
  { const v4u a = {al[2].x, al[2].y, ah[2].x, ah[2].y}; MF16(a1[0][1], a, xf[0][4]); SCHED();
    ah[1] = trd(L + wa0 + 39456); SCHED();
    MF16(a1[1][1], a, xf[1][4]); } SCHED();
  ldc128<1024>(xf[0][9], rw, l16, xb0 + 8192);
  SCHED();
  al[2] = trd(L + wa0 + 35904); SCHED();
  { const v4u a = {al[3].x, al[3].y, ah[3].x, ah[3].y}; MF16(a1[0][2], a, xf[0][4]); SCHED();
    ah[2] = trd(L + wa0 + 39488); SCHED();
    MF16(a1[1][2], a, xf[1][4]); } SCHED();
  al[3] = trd(L + wa0 + 35936); SCHED();
  { const v4u a = {al[4].x, al[4].y, ah[4].x, ah[4].y}; MF16(a1[0][3], a, xf[0][4]); SCHED();
    ah[3] = trd(L + wa0 + 39520); SCHED();
    MF16(a1[1][3], a, xf[1][4]); } SCHED();
  ldc128<1024>(xf[1][9], rw, l16, xb0 + XT + 8192);
  SCHED();
  al[4] = trd(L + wa0 + 43008); SCHED();
  asm volatile("s_waitcnt vmcnt(9)" :  :: "memory");
  { const v4u a = {al[0].x, al[0].y, ah[0].x, ah[0].y}; MF16(a1[0][0], a, xf[0][5]); SCHED();
    ah[4] = trd(L + wa0 + 46592); SCHED();
  asm volatile("s_waitcnt vmcnt(8)" :  :: "memory");
    MF16(a1[1][0], a, xf[1][5]); } SCHED();
  al[0] = trd(L + wa0 + 43040); SCHED();
  { const v4u a = {al[1].x, al[1].y, ah[1].x, ah[1].y}; MF16(a1[0][1], a, xf[0][5]); SCHED();
    ah[0] = trd(L + wa0 + 46624); SCHED();
    MF16(a1[1][1], a, xf[1][5]); } SCHED();
  ldc128<2048>(xf[0][10], rw, l16, xb0 + 8192);
  SCHED();
  al[1] = trd(L + wa0 + 43072); SCHED();
  { const v4u a = {al[2].x, al[2].y, ah[2].x, ah[2].y}; MF16(a1[0][2], a, xf[0][5]); SCHED();
    ah[1] = trd(L + wa0 + 46656); SCHED();
    MF16(a1[1][2], a, xf[1][5]); } SCHED();
  al[2] = trd(L + wa0 + 43104); SCHED();
  { const v4u a = {al[3].x, al[3].y, ah[3].x, ah[3].y}; MF16(a1[0][3], a, xf[0][5]); SCHED();
    ah[2] = trd(L + wa0 + 46688); SCHED();
    MF16(a1[1][3], a, xf[1][5]); } SCHED();
  ldc128<2048>(xf[1][10], rw, l16, xb0 + XT + 8192);
  SCHED();
  al[3] = trd(L + wa0 + 50176); SCHED();
  asm volatile("s_waitcnt vmcnt(9)" :  :: "memory");
  { const v4u a = {al[4].x, al[4].y, ah[4].x, ah[4].y}; MF16(a1[0][0], a, xf[0][6]); SCHED();
    ah[3] = trd(L + wa0 + 53760); SCHED();
  asm volatile("s_waitcnt vmcnt(8)" :  :: "memory");
    MF16(a1[1][0], a, xf[1][6]); } SCHED();
  al[4] = trd(L + wa0 + 50208); SCHED();
  { const v4u a = {al[0].x, al[0].y, ah[0].x, ah[0].y}; MF16(a1[0][1], a, xf[0][6]); SCHED();
    ah[4] = trd(L + wa0 + 53792); SCHED();
    MF16(a1[1][1], a, xf[1][6]); } SCHED();
  ldc128<3072>(xf[0][11], rw, l16, xb0 + 8192);
  SCHED();
  al[0] = trd(L + wa0 + 50240); SCHED();
  { const v4u a = {al[1].x, al[1].y, ah[1].x, ah[1].y}; MF16(a1[0][2], a, xf[0][6]); SCHED();
    ah[0] = trd(L + wa0 + 53824); SCHED();
    MF16(a1[1][2], a, xf[1][6]); } SCHED();
  al[1] = trd(L + wa0 + 50272); SCHED();
  { const v4u a = {al[2].x, al[2].y, ah[2].x, ah[2].y}; MF16(a1[0][3], a, xf[0][6]); SCHED();
    ah[1] = trd(L + wa0 + 53856); SCHED();
    MF16(a1[1][3], a, xf[1][6]); } SCHED();
  ldc128<3072>(xf[1][11], rw, l16, xb0 + XT + 8192);
  SCHED();
  al[2] = trd(L + wa1 + 0); SCHED();
  asm volatile("s_waitcnt vmcnt(9)" :  :: "memory");
  { const v4u a = {al[3].x, al[3].y, ah[3].x, ah[3].y}; MF16(a1[0][0], a, xf[0][7]); SCHED();
    ah[2] = trd(L + wa1 + 3584); SCHED();
  asm volatile("s_waitcnt vmcnt(8)" :  :: "memory");
    MF16(a1[1][0], a, xf[1][7]); } SCHED();
  al[3] = trd(L + wa1 + 32); SCHED();
  { const v4u a = {al[4].x, al[4].y, ah[4].x, ah[4].y}; MF16(a1[0][1], a, xf[0][7]); SCHED();
    ah[3] = trd(L + wa1 + 3616); SCHED();
    MF16(a1[1][1], a, xf[1][7]); } SCHED();
  ldc128<0>(xf[0][12], rw, l16, xb0 + 12288);
  SCHED();
  al[4] = trd(L + wa1 + 64); SCHED();
  { const v4u a = {al[0].x, al[0].y, ah[0].x, ah[0].y}; MF16(a1[0][2], a, xf[0][7]); SCHED();
    ah[4] = trd(L + wa1 + 3648); SCHED();
    MF16(a1[1][2], a, xf[1][7]); } SCHED();
  al[0] = trd(L + wa1 + 96); SCHED();
  { const v4u a = {al[1].x, al[1].y, ah[1].x, ah[1].y}; MF16(a1[0][3], a, xf[0][7]); SCHED();
    ah[0] = trd(L + wa1 + 3680); SCHED();
    MF16(a1[1][3], a, xf[1][7]); } SCHED();
  ldc128<0>(xf[1][12], rw, l16, xb0 + XT + 12288);
  SCHED();
  al[1] = trd(L + wa1 + 7168); SCHED();
  asm volatile("s_waitcnt vmcnt(9)" :  :: "memory");
  { const v4u a = {al[2].x, al[2].y, ah[2].x, ah[2].y}; MF16(a1[0][0], a, xf[0][8]); SCHED();
    ah[1] = trd(L + wa1 + 10752); SCHED();
  asm volatile("s_waitcnt vmcnt(8)" :  :: "memory");
    MF16(a1[1][0], a, xf[1][8]); } SCHED();
  al[2] = trd(L + wa1 + 7200); SCHED();
  { const v4u a = {al[3].x, al[3].y, ah[3].x, ah[3].y}; MF16(a1[0][1], a, xf[0][8]); SCHED();
    ah[2] = trd(L + wa1 + 10784); SCHED();
    MF16(a1[1][1], a, xf[1][8]); } SCHED();
  ldc128<1024>(xf[0][13], rw, l16, xb0 + 12288);
  SCHED();
  al[3] = trd(L + wa1 + 7232); SCHED();
  { const v4u a = {al[4].x, al[4].y, ah[4].x, ah[4].y}; MF16(a1[0][2], a, xf[0][8]); SCHED();
    ah[3] = trd(L + wa1 + 10816); SCHED();
    MF16(a1[1][2], a, xf[1][8]); } SCHED();
  al[4] = trd(L + wa1 + 7264); SCHED();
  { const v4u a = {al[0].x, al[0].y, ah[0].x, ah[0].y}; MF16(a1[0][3], a, xf[0][8]); SCHED();
    ah[4] = trd(L + wa1 + 10848); SCHED();
    MF16(a1[1][3], a, xf[1][8]); } SCHED();
  ldc128<1024>(xf[1][13], rw, l16, xb0 + XT + 12288);
  SCHED();
  al[0] = trd(L + wa1 + 14336); SCHED();
  asm volatile("s_waitcnt vmcnt(9)" :  :: "memory");
  { const v4u a = {al[1].x, al[1].y, ah[1].x, ah[1].y}; MF16(a1[0][0], a, xf[0][9]); SCHED();
    ah[0] = trd(L + wa1 + 17920); SCHED();
  asm volatile("s_waitcnt vmcnt(8)" :  :: "memory");
    MF16(a1[1][0], a, xf[1][9]); } SCHED();
  al[1] = trd(L + wa1 + 14368); SCHED();
  { const v4u a = {al[2].x, al[2].y, ah[2].x, ah[2].y}; MF16(a1[0][1], a, xf[0][9]); SCHED();
    ah[1] = trd(L + wa1 + 17952); SCHED();
    MF16(a1[1][1], a, xf[1][9]); } SCHED();
  ldc128<2048>(xf[0][14], rw, l16, xb0 + 12288);
  SCHED();
  al[2] = trd(L + wa1 + 14400); SCHED();
  { const v4u a = {al[3].x, al[3].y, ah[3].x, ah[3].y}; MF16(a1[0][2], a, xf[0][9]); SCHED();
    ah[2] = trd(L + wa1 + 17984); SCHED();
    MF16(a1[1][2], a, xf[1][9]); } SCHED();
  al[3] = trd(L + wa1 + 14432); SCHED();
  { const v4u a = {al[4].x, al[4].y, ah[4].x, ah[4].y}; MF16(a1[0][3], a, xf[0][9]); SCHED();
    ah[3] = trd(L + wa1 + 18016); SCHED();
    MF16(a1[1][3], a, xf[1][9]); } SCHED();
  ldc128<2048>(xf[1][14], rw, l16, xb0 + XT + 12288);
  SCHED();
  al[4] = trd(L + wa1 + 21504); SCHED();
  asm volatile("s_waitcnt vmcnt(9)" :  :: "memory");
  { const v4u a = {al[0].x, al[0].y, ah[0].x, ah[0].y}; MF16(a1[0][0], a, xf[0][10]); SCHED();
    ah[4] = trd(L + wa1 + 25088); SCHED();
  asm volatile("s_waitcnt vmcnt(8)" :  :: "memory");
    MF16(a1[1][0], a, xf[1][10]); } SCHED();
  al[0] = trd(L + wa1 + 21536); SCHED();
  { const v4u a = {al[1].x, al[1].y, ah[1].x, ah[1].y}; MF16(a1[0][1], a, xf[0][10]); SCHED();
    ah[0] = trd(L + wa1 + 25120); SCHED();
    MF16(a1[1][1], a, xf[1][10]); } SCHED();
  ldc128<3072>(xf[0][15], rw, l16, xb0 + 12288);
  SCHED();
  al[1] = trd(L + wa1 + 21568); SCHED();
  { const v4u a = {al[2].x, al[2].y, ah[2].x, ah[2].y}; MF16(a1[0][2], a, xf[0][10]); SCHED();
    ah[1] = trd(L + wa1 + 25152); SCHED();
    MF16(a1[1][2], a, xf[1][10]); } SCHED();
  al[2] = trd(L + wa1 + 21600); SCHED();
  { const v4u a = {al[3].x, al[3].y, ah[3].x, ah[3].y}; MF16(a1[0][3], a, xf[0][10]); SCHED();
    ah[2] = trd(L + wa1 + 25184); SCHED();
    MF16(a1[1][3], a, xf[1][10]); } SCHED();
  ldc128<3072>(xf[1][15], rw, l16, xb0 + XT + 12288);
  SCHED();
  al[3] = trd(L + wa1 + 28672); SCHED();
  asm volatile("s_waitcnt vmcnt(9)" :  :: "memory");
  { const v4u a = {al[4].x, al[4].y, ah[4].x, ah[4].y}; MF16(a1[0][0], a, xf[0][11]); SCHED();
    ah[3] = trd(L + wa1 + 32256); SCHED();
  asm volatile("s_waitcnt vmcnt(8)" :  :: "memory");
    MF16(a1[1][0], a, xf[1][11]); } SCHED();
  al[4] = trd(L + wa1 + 28704); SCHED();
  { const v4u a = {al[0].x, al[0].y, ah[0].x, ah[0].y}; MF16(a1[0][1], a, xf[0][11]); SCHED();
    ah[4] = trd(L + wa1 + 32288); SCHED();
    MF16(a1[1][1], a, xf[1][11]); } SCHED();
  al[0] = trd(L + wa1 + 28736); SCHED();
  { const v4u a = {al[1].x, al[1].y, ah[1].x, ah[1].y}; MF16(a1[0][2], a, xf[0][11]); SCHED();
    ah[0] = trd(L + wa1 + 32320); SCHED();
    MF16(a1[1][2], a, xf[1][11]); } SCHED();
  al[1] = trd(L + wa1 + 28768); SCHED();
  { const v4u a = {al[2].x, al[2].y, ah[2].x, ah[2].y}; MF16(a1[0][3], a, xf[0][11]); SCHED();
    ah[1] = trd(L + wa1 + 32352); SCHED();
    MF16(a1[1][3], a, xf[1][11]); } SCHED();
  al[2] = trd(L + wa1 + 35840); SCHED();
  asm volatile("s_waitcnt vmcnt(7)" :  :: "memory");
  { const v4u a = {al[3].x, al[3].y, ah[3].x, ah[3].y}; MF16(a1[0][0], a, xf[0][12]); SCHED();
    ah[2] = trd(L + wa1 + 39424); SCHED();
  asm volatile("s_waitcnt vmcnt(6)" :  :: "memory");
    MF16(a1[1][0], a, xf[1][12]); } SCHED();
  al[3] = trd(L + wa1 + 35872); SCHED();
  { const v4u a = {al[4].x, al[4].y, ah[4].x, ah[4].y}; MF16(a1[0][1], a, xf[0][12]); SCHED();
    ah[3] = trd(L + wa1 + 39456); SCHED();
    MF16(a1[1][1], a, xf[1][12]); } SCHED();
  al[4] = trd(L + wa1 + 35904); SCHED();
  { const v4u a = {al[0].x, al[0].y, ah[0].x, ah[0].y}; MF16(a1[0][2], a, xf[0][12]); SCHED();
    ah[4] = trd(L + wa1 + 39488); SCHED();
    MF16(a1[1][2], a, xf[1][12]); } SCHED();
  al[0] = trd(L + wa1 + 35936); SCHED();
  { const v4u a = {al[1].x, al[1].y, ah[1].x, ah[1].y}; MF16(a1[0][3], a, xf[0][12]); SCHED();
    ah[0] = trd(L + wa1 + 39520); SCHED();
    MF16(a1[1][3], a, xf[1][12]); } SCHED();
  al[1] = trd(L + wa1 + 43008); SCHED();
  asm volatile("s_waitcnt vmcnt(5)" :  :: "memory");
  { const v4u a = {al[2].x, al[2].y, ah[2].x, ah[2].y}; MF16(a1[0][0], a, xf[0][13]); SCHED();
    ah[1] = trd(L + wa1 + 46592); SCHED();
  asm volatile("s_waitcnt vmcnt(4)" :  :: "memory");
    MF16(a1[1][0], a, xf[1][13]); } SCHED();
  al[2] = trd(L + wa1 + 43040); SCHED();
  { const v4u a = {al[3].x, al[3].y, ah[3].x, ah[3].y}; MF16(a1[0][1], a, xf[0][13]); SCHED();
    ah[2] = trd(L + wa1 + 46624); SCHED();
    MF16(a1[1][1], a, xf[1][13]); } SCHED();
  al[3] = trd(L + wa1 + 43072); SCHED();
  { const v4u a = {al[4].x, al[4].y, ah[4].x, ah[4].y}; MF16(a1[0][2], a, xf[0][13]); SCHED();
    ah[3] = trd(L + wa1 + 46656); SCHED();
    MF16(a1[1][2], a, xf[1][13]); } SCHED();
  al[4] = trd(L + wa1 + 43104); SCHED();
  { const v4u a = {al[0].x, al[0].y, ah[0].x, ah[0].y}; MF16(a1[0][3], a, xf[0][13]); SCHED();
    ah[4] = trd(L + wa1 + 46688); SCHED();
    MF16(a1[1][3], a, xf[1][13]); } SCHED();
  al[0] = trd(L + wa1 + 50176); SCHED();
  asm volatile("s_waitcnt vmcnt(3)" :  :: "memory");
  { const v4u a = {al[1].x, al[1].y, ah[1].x, ah[1].y}; MF16(a1[0][0], a, xf[0][14]); SCHED();
    ah[0] = trd(L + wa1 + 53760); SCHED();
  asm volatile("s_waitcnt vmcnt(2)" :  :: "memory");
    MF16(a1[1][0], a, xf[1][14]); } SCHED();
  al[1] = trd(L + wa1 + 50208); SCHED();
  { const v4u a = {al[2].x, al[2].y, ah[2].x, ah[2].y}; MF16(a1[0][1], a, xf[0][14]); SCHED();
    ah[1] = trd(L + wa1 + 53792); SCHED();
    MF16(a1[1][1], a, xf[1][14]); } SCHED();
  al[2] = trd(L + wa1 + 50240); SCHED();
  { const v4u a = {al[3].x, al[3].y, ah[3].x, ah[3].y}; MF16(a1[0][2], a, xf[0][14]); SCHED();
    ah[2] = trd(L + wa1 + 53824); SCHED();
    MF16(a1[1][2], a, xf[1][14]); } SCHED();
  al[3] = trd(L + wa1 + 50272); SCHED();
  { const v4u a = {al[4].x, al[4].y, ah[4].x, ah[4].y}; MF16(a1[0][3], a, xf[0][14]); SCHED();
    ah[3] = trd(L + wa1 + 53856); SCHED();
    MF16(a1[1][3], a, xf[1][14]); } SCHED();
  al[4] = trd(L + wa0 + 128); SCHED();
  asm volatile("s_waitcnt vmcnt(1)" :  :: "memory");
  { const v4u a = {al[0].x, al[0].y, ah[0].x, ah[0].y}; MF16(a1[0][0], a, xf[0][15]); SCHED();
    ah[4] = trd(L + wa0 + 3712); SCHED();
  asm volatile("s_waitcnt vmcnt(0)" :  :: "memory");
    MF16(a1[1][0], a, xf[1][15]); } SCHED();
  al[0] = trd(L + wa0 + 160); SCHED();
  { const v4u a = {al[1].x, al[1].y, ah[1].x, ah[1].y}; MF16(a1[0][1], a, xf[0][15]); SCHED();
    ah[0] = trd(L + wa0 + 3744); SCHED();
    MF16(a1[1][1], a, xf[1][15]); } SCHED();
  al[1] = trd(L + wa0 + 192); SCHED();
  { const v4u a = {al[2].x, al[2].y, ah[2].x, ah[2].y}; MF16(a1[0][2], a, xf[0][15]); SCHED();
    ah[1] = trd(L + wa0 + 3776); SCHED();
    MF16(a1[1][2], a, xf[1][15]); } SCHED();
  al[2] = trd(L + wa0 + 7296); SCHED();
  { const v4u a = {al[3].x, al[3].y, ah[3].x, ah[3].y}; MF16(a1[0][3], a, xf[0][15]); SCHED();
    ah[2] = trd(L + wa0 + 10880); SCHED();
    MF16(a1[1][3], a, xf[1][15]); } SCHED();
  { v4u xe0 = {mh[0], mh[0], sh[0], 0u}, xe1 = {mh[1], mh[1], sh[1], 0u}; PIN(xe0); PIN(xe1); NOPS(1);
    MF16A(a1[0][0], we[0], xe0); MF16A(a1[1][0], we[0], xe1);
    MF16A(a1[0][1], we[1], xe0); MF16A(a1[1][1], we[1], xe1);
    MF16A(a1[0][2], we[2], xe0); MF16A(a1[1][2], we[2], xe1);
    MF16A(a1[0][3], we[3], xe0); MF16A(a1[1][3], we[3], xe1);
  }
  NOPS(7);
  gelu8(a1[0][0], a1[0][1], gc[0], gd[0], gc[0], gd[0], go);
  st128(v4u{go[0], go[1], go[2], go[3]}, rw, l16 + 0, hp0); SCHED();
  gelu8(a1[1][0], a1[1][1], gc[1], gd[1], gc[1], gd[1], go);
  st128(v4u{go[0], go[1], go[2], go[3]}, rw, l16 + 0, hp0 + HT); SCHED();
  gelu8(a1[0][2], a1[0][3], gc[0], gd[0], gc[0], gd[0], go);
  st128(v4u{go[0], go[1], go[2], go[3]}, rw, l16 + 1024, hp0); SCHED();
  gelu8(a1[1][2], a1[1][3], gc[1], gd[1], gc[1], gd[1], go);
  st128(v4u{go[0], go[1], go[2], go[3]}, rw, l16 + 1024, hp0 + HT); SCHED();
  al[3] = trd(L + wa0 + 7328); SCHED();
  { const v4u a = {al[4].x, al[4].y, ah[4].x, ah[4].y}; MF16Z(a1[0][4], a, xf[0][0]); SCHED();
    ah[3] = trd(L + wa0 + 10912); SCHED();
    MF16Z(a1[1][4], a, xf[1][0]); } SCHED();
  al[4] = trd(L + wa0 + 7360); SCHED();
  { const v4u a = {al[0].x, al[0].y, ah[0].x, ah[0].y}; MF16Z(a1[0][5], a, xf[0][0]); SCHED();
    ah[4] = trd(L + wa0 + 10944); SCHED();
    MF16Z(a1[1][5], a, xf[1][0]); } SCHED();
  LDA128(w2f[6][0], rw, l16, w2s + 24576, 0);
  SCHED();
  al[0] = trd(L + wa0 + 14464); SCHED();
  { const v4u a = {al[1].x, al[1].y, ah[1].x, ah[1].y}; MF16Z(a1[0][6], a, xf[0][0]); SCHED();
    ah[0] = trd(L + wa0 + 18048); SCHED();
    MF16Z(a1[1][6], a, xf[1][0]); } SCHED();
  al[1] = trd(L + wa0 + 14496); SCHED();
  { const v4u a = {al[2].x, al[2].y, ah[2].x, ah[2].y}; MF16(a1[0][4], a, xf[0][1]); SCHED();
    ah[1] = trd(L + wa0 + 18080); SCHED();
    MF16(a1[1][4], a, xf[1][1]); } SCHED();
  LDA128(w2f[6][1], rw, l16, w2s + 24576, 1024);
  SCHED();
  al[2] = trd(L + wa0 + 14528); SCHED();
  { const v4u a = {al[3].x, al[3].y, ah[3].x, ah[3].y}; MF16(a1[0][5], a, xf[0][1]); SCHED();
    ah[2] = trd(L + wa0 + 18112); SCHED();
    MF16(a1[1][5], a, xf[1][1]); } SCHED();
  al[3] = trd(L + wa0 + 21632); SCHED();
  { const v4u a = {al[4].x, al[4].y, ah[4].x, ah[4].y}; MF16(a1[0][6], a, xf[0][1]); SCHED();
    ah[3] = trd(L + wa0 + 25216); SCHED();
    MF16(a1[1][6], a, xf[1][1]); } SCHED();
  LDA128(w2f[6][2], rw, l16, w2s + 24576, 2048);
  SCHED();
  al[4] = trd(L + wa0 + 21664); SCHED();
  { const v4u a = {al[0].x, al[0].y, ah[0].x, ah[0].y}; MF16(a1[0][4], a, xf[0][2]); SCHED();
    ah[4] = trd(L + wa0 + 25248); SCHED();
    MF16(a1[1][4], a, xf[1][2]); } SCHED();
  al[0] = trd(L + wa0 + 21696); SCHED();
  { const v4u a = {al[1].x, al[1].y, ah[1].x, ah[1].y}; MF16(a1[0][5], a, xf[0][2]); SCHED();
    ah[0] = trd(L + wa0 + 25280); SCHED();
    MF16(a1[1][5], a, xf[1][2]); } SCHED();
  LDA128(w2f[6][3], rw, l16, w2s + 24576, 3072);
  SCHED();
  al[1] = trd(L + wa0 + 28800); SCHED();
  { const v4u a = {al[2].x, al[2].y, ah[2].x, ah[2].y}; MF16(a1[0][6], a, xf[0][2]); SCHED();
    ah[1] = trd(L + wa0 + 32384); SCHED();
    MF16(a1[1][6], a, xf[1][2]); } SCHED();
  al[2] = trd(L + wa0 + 28832); SCHED();
  { const v4u a = {al[3].x, al[3].y, ah[3].x, ah[3].y}; MF16(a1[0][4], a, xf[0][3]); SCHED();
    ah[2] = trd(L + wa0 + 32416); SCHED();
    MF16(a1[1][4], a, xf[1][3]); } SCHED();
  LDA128(w2f[7][0], rw, l16, w2s + 28672, 0);
  SCHED();
  al[3] = trd(L + wa0 + 28864); SCHED();
  { const v4u a = {al[4].x, al[4].y, ah[4].x, ah[4].y}; MF16(a1[0][5], a, xf[0][3]); SCHED();
    ah[3] = trd(L + wa0 + 32448); SCHED();
    MF16(a1[1][5], a, xf[1][3]); } SCHED();
  al[4] = trd(L + wa0 + 35968); SCHED();
  { const v4u a = {al[0].x, al[0].y, ah[0].x, ah[0].y}; MF16(a1[0][6], a, xf[0][3]); SCHED();
    ah[4] = trd(L + wa0 + 39552); SCHED();
    MF16(a1[1][6], a, xf[1][3]); } SCHED();
  LDA128(w2f[7][1], rw, l16, w2s + 28672, 1024);
  SCHED();
  al[0] = trd(L + wa0 + 36000); SCHED();
  { const v4u a = {al[1].x, al[1].y, ah[1].x, ah[1].y}; MF16(a1[0][4], a, xf[0][4]); SCHED();
    ah[0] = trd(L + wa0 + 39584); SCHED();
    MF16(a1[1][4], a, xf[1][4]); } SCHED();
  al[1] = trd(L + wa0 + 36032); SCHED();
  { const v4u a = {al[2].x, al[2].y, ah[2].x, ah[2].y}; MF16(a1[0][5], a, xf[0][4]); SCHED();
    ah[1] = trd(L + wa0 + 39616); SCHED();
    MF16(a1[1][5], a, xf[1][4]); } SCHED();
  LDA128(w2f[7][2], rw, l16, w2s + 28672, 2048);
  SCHED();
  al[2] = trd(L + wa0 + 43136); SCHED();
  { const v4u a = {al[3].x, al[3].y, ah[3].x, ah[3].y}; MF16(a1[0][6], a, xf[0][4]); SCHED();
    ah[2] = trd(L + wa0 + 46720); SCHED();
    MF16(a1[1][6], a, xf[1][4]); } SCHED();
  al[3] = trd(L + wa0 + 43168); SCHED();
  { const v4u a = {al[4].x, al[4].y, ah[4].x, ah[4].y}; MF16(a1[0][4], a, xf[0][5]); SCHED();
    ah[3] = trd(L + wa0 + 46752); SCHED();
    MF16(a1[1][4], a, xf[1][5]); } SCHED();
  LDA128(w2f[7][3], rw, l16, w2s + 28672, 3072);
  SCHED();
  al[4] = trd(L + wa0 + 43200); SCHED();
  { const v4u a = {al[0].x, al[0].y, ah[0].x, ah[0].y}; MF16(a1[0][5], a, xf[0][5]); SCHED();
    ah[4] = trd(L + wa0 + 46784); SCHED();
    MF16(a1[1][5], a, xf[1][5]); } SCHED();
  al[0] = trd(L + wa0 + 50304); SCHED();
  { const v4u a = {al[1].x, al[1].y, ah[1].x, ah[1].y}; MF16(a1[0][6], a, xf[0][5]); SCHED();
    ah[0] = trd(L + wa0 + 53888); SCHED();
    MF16(a1[1][6], a, xf[1][5]); } SCHED();
  LDA128(w2f[8][0], rw, l16, w2s + 32768, 0);
  SCHED();
  al[1] = trd(L + wa0 + 50336); SCHED();
  { const v4u a = {al[2].x, al[2].y, ah[2].x, ah[2].y}; MF16(a1[0][4], a, xf[0][6]); SCHED();
    ah[1] = trd(L + wa0 + 53920); SCHED();
    MF16(a1[1][4], a, xf[1][6]); } SCHED();
  al[2] = trd(L + wa0 + 50368); SCHED();
  { const v4u a = {al[3].x, al[3].y, ah[3].x, ah[3].y}; MF16(a1[0][5], a, xf[0][6]); SCHED();
    ah[2] = trd(L + wa0 + 53952); SCHED();
    MF16(a1[1][5], a, xf[1][6]); } SCHED();
  LDA128(w2f[8][1], rw, l16, w2s + 32768, 1024);
  SCHED();
  al[3] = trd(L + wa1 + 128); SCHED();
  { const v4u a = {al[4].x, al[4].y, ah[4].x, ah[4].y}; MF16(a1[0][6], a, xf[0][6]); SCHED();
    ah[3] = trd(L + wa1 + 3712); SCHED();
    MF16(a1[1][6], a, xf[1][6]); } SCHED();
  al[4] = trd(L + wa1 + 160); SCHED();
  { const v4u a = {al[0].x, al[0].y, ah[0].x, ah[0].y}; MF16(a1[0][4], a, xf[0][7]); SCHED();
    ah[4] = trd(L + wa1 + 3744); SCHED();
    MF16(a1[1][4], a, xf[1][7]); } SCHED();
  LDA128(w2f[8][2], rw, l16, w2s + 32768, 2048);
  SCHED();
  al[0] = trd(L + wa1 + 192); SCHED();
  { const v4u a = {al[1].x, al[1].y, ah[1].x, ah[1].y}; MF16(a1[0][5], a, xf[0][7]); SCHED();
    ah[0] = trd(L + wa1 + 3776); SCHED();
    MF16(a1[1][5], a, xf[1][7]); } SCHED();
  asm volatile("s_waitcnt vmcnt(11)" :  :: "memory");
  __builtin_amdgcn_s_barrier();
  if (w == 0) set_flag(rw, flm + 0 * FLAG_ARR, tag, lane);
  al[1] = trd(L + wa1 + 7296); SCHED();
  { const v4u a = {al[2].x, al[2].y, ah[2].x, ah[2].y}; MF16(a1[0][6], a, xf[0][7]); SCHED();
    ah[1] = trd(L + wa1 + 10880); SCHED();
    MF16(a1[1][6], a, xf[1][7]); } SCHED();
  LDA128(w2f[8][3], rw, l16, w2s + 32768, 3072);
  SCHED();
  al[2] = trd(L + wa1 + 7328); SCHED();
  { const v4u a = {al[3].x, al[3].y, ah[3].x, ah[3].y}; MF16(a1[0][4], a, xf[0][8]); SCHED();
    ah[2] = trd(L + wa1 + 10912); SCHED();
    MF16(a1[1][4], a, xf[1][8]); } SCHED();
  al[3] = trd(L + wa1 + 7360); SCHED();
  { const v4u a = {al[4].x, al[4].y, ah[4].x, ah[4].y}; MF16(a1[0][5], a, xf[0][8]); SCHED();
    ah[3] = trd(L + wa1 + 10944); SCHED();
    MF16(a1[1][5], a, xf[1][8]); } SCHED();
  LDA128(w2f[9][0], rw, l16, w2s + 36864, 0);
  SCHED();
  al[4] = trd(L + wa1 + 14464); SCHED();
  { const v4u a = {al[0].x, al[0].y, ah[0].x, ah[0].y}; MF16(a1[0][6], a, xf[0][8]); SCHED();
    ah[4] = trd(L + wa1 + 18048); SCHED();
    MF16(a1[1][6], a, xf[1][8]); } SCHED();
  al[0] = trd(L + wa1 + 14496); SCHED();
  { const v4u a = {al[1].x, al[1].y, ah[1].x, ah[1].y}; MF16(a1[0][4], a, xf[0][9]); SCHED();
    ah[0] = trd(L + wa1 + 18080); SCHED();
    MF16(a1[1][4], a, xf[1][9]); } SCHED();
  LDA128(w2f[9][1], rw, l16, w2s + 36864, 1024);
  SCHED();
  al[1] = trd(L + wa1 + 14528); SCHED();
  { const v4u a = {al[2].x, al[2].y, ah[2].x, ah[2].y}; MF16(a1[0][5], a, xf[0][9]); SCHED();
    ah[1] = trd(L + wa1 + 18112); SCHED();
    MF16(a1[1][5], a, xf[1][9]); } SCHED();
  al[2] = trd(L + wa1 + 21632); SCHED();
  { const v4u a = {al[3].x, al[3].y, ah[3].x, ah[3].y}; MF16(a1[0][6], a, xf[0][9]); SCHED();
    ah[2] = trd(L + wa1 + 25216); SCHED();
    MF16(a1[1][6], a, xf[1][9]); } SCHED();
  LDA128(w2f[9][2], rw, l16, w2s + 36864, 2048);
  SCHED();
  al[3] = trd(L + wa1 + 21664); SCHED();
  { const v4u a = {al[4].x, al[4].y, ah[4].x, ah[4].y}; MF16(a1[0][4], a, xf[0][10]); SCHED();
    ah[3] = trd(L + wa1 + 25248); SCHED();
    MF16(a1[1][4], a, xf[1][10]); } SCHED();
  al[4] = trd(L + wa1 + 21696); SCHED();
  { const v4u a = {al[0].x, al[0].y, ah[0].x, ah[0].y}; MF16(a1[0][5], a, xf[0][10]); SCHED();
    ah[4] = trd(L + wa1 + 25280); SCHED();
    MF16(a1[1][5], a, xf[1][10]); } SCHED();
  LDA128(w2f[9][3], rw, l16, w2s + 36864, 3072);
  SCHED();
  al[0] = trd(L + wa1 + 28800); SCHED();
  { const v4u a = {al[1].x, al[1].y, ah[1].x, ah[1].y}; MF16(a1[0][6], a, xf[0][10]); SCHED();
    ah[0] = trd(L + wa1 + 32384); SCHED();
    MF16(a1[1][6], a, xf[1][10]); } SCHED();
  al[1] = trd(L + wa1 + 28832); SCHED();
  { const v4u a = {al[2].x, al[2].y, ah[2].x, ah[2].y}; MF16(a1[0][4], a, xf[0][11]); SCHED();
    ah[1] = trd(L + wa1 + 32416); SCHED();
    MF16(a1[1][4], a, xf[1][11]); } SCHED();
  LDA128(w2f[10][0], rw, l16, w2s + 40960, 0);
  SCHED();
  al[2] = trd(L + wa1 + 28864); SCHED();
  { const v4u a = {al[3].x, al[3].y, ah[3].x, ah[3].y}; MF16(a1[0][5], a, xf[0][11]); SCHED();
    ah[2] = trd(L + wa1 + 32448); SCHED();
    MF16(a1[1][5], a, xf[1][11]); } SCHED();
  al[3] = trd(L + wa1 + 35968); SCHED();
  { const v4u a = {al[4].x, al[4].y, ah[4].x, ah[4].y}; MF16(a1[0][6], a, xf[0][11]); SCHED();
    ah[3] = trd(L + wa1 + 39552); SCHED();
    MF16(a1[1][6], a, xf[1][11]); } SCHED();
  LDA128(w2f[10][1], rw, l16, w2s + 40960, 1024);
  SCHED();
  al[4] = trd(L + wa1 + 36000); SCHED();
  { const v4u a = {al[0].x, al[0].y, ah[0].x, ah[0].y}; MF16(a1[0][4], a, xf[0][12]); SCHED();
    ah[4] = trd(L + wa1 + 39584); SCHED();
    MF16(a1[1][4], a, xf[1][12]); } SCHED();
  al[0] = trd(L + wa1 + 36032); SCHED();
  { const v4u a = {al[1].x, al[1].y, ah[1].x, ah[1].y}; MF16(a1[0][5], a, xf[0][12]); SCHED();
    ah[0] = trd(L + wa1 + 39616); SCHED();
    MF16(a1[1][5], a, xf[1][12]); } SCHED();
  LDA128(w2f[10][2], rw, l16, w2s + 40960, 2048);
  SCHED();
  al[1] = trd(L + wa1 + 43136); SCHED();
  { const v4u a = {al[2].x, al[2].y, ah[2].x, ah[2].y}; MF16(a1[0][6], a, xf[0][12]); SCHED();
    ah[1] = trd(L + wa1 + 46720); SCHED();
    MF16(a1[1][6], a, xf[1][12]); } SCHED();
  al[2] = trd(L + wa1 + 43168); SCHED();
  { const v4u a = {al[3].x, al[3].y, ah[3].x, ah[3].y}; MF16(a1[0][4], a, xf[0][13]); SCHED();
    ah[2] = trd(L + wa1 + 46752); SCHED();
    MF16(a1[1][4], a, xf[1][13]); } SCHED();
  LDA128(w2f[10][3], rw, l16, w2s + 40960, 3072);
  SCHED();
  al[3] = trd(L + wa1 + 43200); SCHED();
  { const v4u a = {al[4].x, al[4].y, ah[4].x, ah[4].y}; MF16(a1[0][5], a, xf[0][13]); SCHED();
    ah[3] = trd(L + wa1 + 46784); SCHED();
    MF16(a1[1][5], a, xf[1][13]); } SCHED();
  al[4] = trd(L + wa1 + 50304); SCHED();
  { const v4u a = {al[0].x, al[0].y, ah[0].x, ah[0].y}; MF16(a1[0][6], a, xf[0][13]); SCHED();
    ah[4] = trd(L + wa1 + 53888); SCHED();
    MF16(a1[1][6], a, xf[1][13]); } SCHED();
  LDA128(w2f[11][0], rw, l16, w2s + 45056, 0);
  SCHED();
  al[0] = trd(L + wa1 + 50336); SCHED();
  { const v4u a = {al[1].x, al[1].y, ah[1].x, ah[1].y}; MF16(a1[0][4], a, xf[0][14]); SCHED();
    ah[0] = trd(L + wa1 + 53920); SCHED();
    MF16(a1[1][4], a, xf[1][14]); } SCHED();
  al[1] = trd(L + wa1 + 50368); SCHED();
  { const v4u a = {al[2].x, al[2].y, ah[2].x, ah[2].y}; MF16(a1[0][5], a, xf[0][14]); SCHED();
    ah[1] = trd(L + wa1 + 53952); SCHED();
    MF16(a1[1][5], a, xf[1][14]); } SCHED();
  LDA128(w2f[11][1], rw, l16, w2s + 45056, 1024);
  SCHED();
  { const v4u a = {al[3].x, al[3].y, ah[3].x, ah[3].y}; MF16(a1[0][6], a, xf[0][14]); SCHED();
    MF16(a1[1][6], a, xf[1][14]); } SCHED();
  { const v4u a = {al[4].x, al[4].y, ah[4].x, ah[4].y}; MF16(a1[0][4], a, xf[0][15]); SCHED();
    MF16(a1[1][4], a, xf[1][15]); } SCHED();
  LDA128(w2f[11][2], rw, l16, w2s + 45056, 2048);
  SCHED();
  { const v4u a = {al[0].x, al[0].y, ah[0].x, ah[0].y}; MF16(a1[0][5], a, xf[0][15]); SCHED();
    MF16(a1[1][5], a, xf[1][15]); } SCHED();
  { const v4u a = {al[1].x, al[1].y, ah[1].x, ah[1].y}; MF16(a1[0][6], a, xf[0][15]); SCHED();
    MF16(a1[1][6], a, xf[1][15]); } SCHED();
  LDA128(w2f[11][3], rw, l16, w2s + 45056, 3072);
  SCHED();
  LDA128(w2f[12][0], rw, l16, w2s + 49152, 0);
  LDA128(w2f[12][1], rw, l16, w2s + 49152, 1024);
  LDA128(w2f[12][2], rw, l16, w2s + 49152, 2048);
  LDA128(w2f[12][3], rw, l16, w2s + 49152, 3072);
  LDA128(w2f[13][0], rw, l16, w2s + 53248, 0);
  LDA128(w2f[13][1], rw, l16, w2s + 53248, 1024);
  LDA128(w2f[13][2], rw, l16, w2s + 53248, 2048);
  LDA128(w2f[13][3], rw, l16, w2s + 53248, 3072);
  { v4u xe0 = {mh[0], mh[0], sh[0], 0u}, xe1 = {mh[1], mh[1], sh[1], 0u}; PIN(xe0); PIN(xe1); NOPS(1);
    MF16A(a1[0][4], we[4], xe0); MF16A(a1[1][4], we[4], xe1);
    MF16A(a1[0][5], we[5], xe0); MF16A(a1[1][5], we[5], xe1);
    MF16A(a1[0][6], we[6], xe0); MF16A(a1[1][6], we[6], xe1);
  }
  NOPS(7);
  ldflag(fh, rw, fo, flw + 0 * FLAG_ARR);
  gelu8(a1[0][4], a1[0][5], gc[0], gd[0], gc[0], gd[0], go);
  st128(v4u{go[0], go[1], go[2], go[3]}, rw, l16 + 2048, hp0); SCHED();
  gelu8(a1[1][4], a1[1][5], gc[1], gd[1], gc[1], gd[1], go);
  st128(v4u{go[0], go[1], go[2], go[3]}, rw, l16 + 2048, hp0 + HT); SCHED();
  gelu8(a1[0][6], a1[1][6], gc[0], gd[0], gc[1], gd[1], go); swap_rows(go);
  st128(v4u{go[0], go[1], go[2], go[3]}, rw, h6o, hp60); SCHED();
  asm volatile("s_waitcnt vmcnt(3)" : "+v"(fh) :: "memory");
  if (__builtin_expect(!all_eq(fh, tag), 0)) poll(rw, fo, flw + 0 * FLAG_ARR, tag);
  ldc128<0>(hf[0][0], rw, l16, hq0[0] + 0);
  ldc128<0>(hf[0][1], rw, l16, hq0[1] + 0);
  ldc128<0>(hf[0][2], rw, l16, hq0[2] + 0);
  ldc128<0>(hf[0][3], rw, l16, hq0[3] + 0);
  ldc128<1024>(hf[1][0], rw, l16, hq0[0] + 0);
  ldc128<1024>(hf[1][1], rw, l16, hq0[1] + 0);
  ldc128<1024>(hf[1][2], rw, l16, hq0[2] + 0);
  ldc128<1024>(hf[1][3], rw, l16, hq0[3] + 0);
  ldc128<0>(hf[4][0], rw, l16, hq0[0] + 4096);
  ldc128<0>(hf[4][1], rw, l16, hq0[1] + 4096);
  ldc128<0>(hf[4][2], rw, l16, hq0[2] + 4096);
  ldc128<0>(hf[4][3], rw, l16, hq0[3] + 4096);
  SCHED();
  asm volatile("" : "+v"(b2p[0]), "+v"(b2p[1]));
  residual(a2[3], xr, b2p); NOPS(1);
  asm volatile("s_waitcnt vmcnt(12)" :  :: "memory");
  __builtin_amdgcn_s_barrier();
  if (w == 0) set_flag(rw, flm + 1 * FLAG_ARR, tag, lane);
  asm volatile("s_waitcnt vmcnt(11)" :  :: "memory");
  MF16A0(a2[0][0], w2f[0][0], hf[0][0]);
  ldc128<1024>(hf[5][0], rw, l16, hq0[0] + 4096);
  MF16A0(a2[0][1], w2f[0][1], hf[0][0]);
  MF16A0(a2[0][2], w2f[0][2], hf[0][0]);
  asm volatile("s_waitcnt vmcnt(11)" :  :: "memory");
  MF16A0(a2[0][3], w2f[0][3], hf[0][0]);
  SCHED();
  MF16A0(a2[1][0], w2f[0][0], hf[0][1]);
  ldc128<1024>(hf[5][1], rw, l16, hq0[1] + 4096);
  MF16A0(a2[1][1], w2f[0][1], hf[0][1]);
  MF16A0(a2[1][2], w2f[0][2], hf[0][1]);
  asm volatile("s_waitcnt vmcnt(11)" :  :: "memory");
  MF16A0(a2[1][3], w2f[0][3], hf[0][1]);
  SCHED();
  MF16A0(a2[2][0], w2f[0][0], hf[0][2]);
  ldc128<1024>(hf[5][2], rw, l16, hq0[2] + 4096);
  MF16A0(a2[2][1], w2f[0][1], hf[0][2]);
  MF16A0(a2[2][2], w2f[0][2], hf[0][2]);
  asm volatile("s_waitcnt vmcnt(11)" :  :: "memory");
  MF16A0(a2[2][3], w2f[0][3], hf[0][2]);
  SCHED();
  MF16A(a2[3][0], w2f[0][0], hf[0][3]);
  ldc128<1024>(hf[5][3], rw, l16, hq0[3] + 4096);
  MF16A(a2[3][1], w2f[0][1], hf[0][3]);
  MF16A(a2[3][2], w2f[0][2], hf[0][3]);
  asm volatile("s_waitcnt vmcnt(11)" :  :: "memory");
  MF16A(a2[3][3], w2f[0][3], hf[0][3]);
  SCHED();
  MF16A(a2[0][0], w2f[1][0], hf[1][0]);
  ldc128<3072>(hf[7][0], rw, l16, hq0[0] + 4096);
  MF16A(a2[0][1], w2f[1][1], hf[1][0]);
  MF16A(a2[0][2], w2f[1][2], hf[1][0]);
  asm volatile("s_waitcnt vmcnt(11)" :  :: "memory");
  MF16A(a2[0][3], w2f[1][3], hf[1][0]);
  SCHED();
  MF16A(a2[1][0], w2f[1][0], hf[1][1]);
  ldc128<3072>(hf[7][1], rw, l16, hq0[1] + 4096);
  MF16A(a2[1][1], w2f[1][1], hf[1][1]);
  MF16A(a2[1][2], w2f[1][2], hf[1][1]);
  asm volatile("s_waitcnt vmcnt(11)" :  :: "memory");
  MF16A(a2[1][3], w2f[1][3], hf[1][1]);
  SCHED();
  MF16A(a2[2][0], w2f[1][0], hf[1][2]);
  ldc128<3072>(hf[7][2], rw, l16, hq0[2] + 4096);
  MF16A(a2[2][1], w2f[1][1], hf[1][2]);
  MF16A(a2[2][2], w2f[1][2], hf[1][2]);
  asm volatile("s_waitcnt vmcnt(11)" :  :: "memory");
  MF16A(a2[2][3], w2f[1][3], hf[1][2]);
  SCHED();
  MF16A(a2[3][0], w2f[1][0], hf[1][3]);
  ldc128<3072>(hf[7][3], rw, l16, hq0[3] + 4096);
  MF16A(a2[3][1], w2f[1][1], hf[1][3]);
  MF16A(a2[3][2], w2f[1][2], hf[1][3]);
  asm volatile("s_waitcnt vmcnt(11)" :  :: "memory");
  MF16A(a2[3][3], w2f[1][3], hf[1][3]);
  SCHED();
  MF16A(a2[0][0], w2f[4][0], hf[4][0]);
  ldc128<0>(hf[8][0], rw, l16, hq0[0] + 8192);
  MF16A(a2[0][1], w2f[4][1], hf[4][0]);
  MF16A(a2[0][2], w2f[4][2], hf[4][0]);
  asm volatile("s_waitcnt vmcnt(11)" :  :: "memory");
  MF16A(a2[0][3], w2f[4][3], hf[4][0]);
  SCHED();
  MF16A(a2[1][0], w2f[4][0], hf[4][1]);
  ldc128<0>(hf[8][1], rw, l16, hq0[1] + 8192);
  MF16A(a2[1][1], w2f[4][1], hf[4][1]);
  MF16A(a2[1][2], w2f[4][2], hf[4][1]);
  asm volatile("s_waitcnt vmcnt(11)" :  :: "memory");
  MF16A(a2[1][3], w2f[4][3], hf[4][1]);
  SCHED();
  MF16A(a2[2][0], w2f[4][0], hf[4][2]);
  ldc128<0>(hf[8][2], rw, l16, hq0[2] + 8192);
  MF16A(a2[2][1], w2f[4][1], hf[4][2]);
  MF16A(a2[2][2], w2f[4][2], hf[4][2]);
  asm volatile("s_waitcnt vmcnt(11)" :  :: "memory");
  MF16A(a2[2][3], w2f[4][3], hf[4][2]);
  SCHED();
  MF16A(a2[3][0], w2f[4][0], hf[4][3]);
  ldc128<0>(hf[8][3], rw, l16, hq0[3] + 8192);
  MF16A(a2[3][1], w2f[4][1], hf[4][3]);
  MF16A(a2[3][2], w2f[4][2], hf[4][3]);
  asm volatile("s_waitcnt vmcnt(11)" :  :: "memory");
  MF16A(a2[3][3], w2f[4][3], hf[4][3]);
  SCHED();
  MF16A(a2[0][0], w2f[5][0], hf[5][0]);
  ldc128<3072>(hf[11][0], rw, l16, hq0[0] + 8192);
  MF16A(a2[0][1], w2f[5][1], hf[5][0]);
  MF16A(a2[0][2], w2f[5][2], hf[5][0]);
  asm volatile("s_waitcnt vmcnt(11)" :  :: "memory");
  MF16A(a2[0][3], w2f[5][3], hf[5][0]);
  SCHED();
  ldflag(fh, rw, fo, flw + 1 * FLAG_ARR);
  MF16A(a2[1][0], w2f[5][0], hf[5][1]);
  ldc128<3072>(hf[11][1], rw, l16, hq0[1] + 8192);
  MF16A(a2[1][1], w2f[5][1], hf[5][1]);
  MF16A(a2[1][2], w2f[5][2], hf[5][1]);
  asm volatile("s_waitcnt vmcnt(12)" :  :: "memory");
  MF16A(a2[1][3], w2f[5][3], hf[5][1]);
  SCHED();
  MF16A(a2[2][0], w2f[5][0], hf[5][2]);
  ldc128<3072>(hf[11][2], rw, l16, hq0[2] + 8192);
  MF16A(a2[2][1], w2f[5][1], hf[5][2]);
  MF16A(a2[2][2], w2f[5][2], hf[5][2]);
  asm volatile("s_waitcnt vmcnt(12)" :  :: "memory");
  MF16A(a2[2][3], w2f[5][3], hf[5][2]);
  SCHED();
  MF16A(a2[3][0], w2f[5][0], hf[5][3]);
  ldc128<3072>(hf[11][3], rw, l16, hq0[3] + 8192);
  MF16A(a2[3][1], w2f[5][1], hf[5][3]);
  MF16A(a2[3][2], w2f[5][2], hf[5][3]);
  asm volatile("s_waitcnt vmcnt(12)" :  :: "memory");
  MF16A(a2[3][3], w2f[5][3], hf[5][3]);
  SCHED();
  MF16A(a2[0][0], w2f[7][0], hf[7][0]);
  ldc128<0>(hf[12][0], rw, l16, hq0[0] + 12288);
  MF16A(a2[0][1], w2f[7][1], hf[7][0]);
  MF16A(a2[0][2], w2f[7][2], hf[7][0]);
  asm volatile("s_waitcnt vmcnt(12)" :  :: "memory");
  MF16A(a2[0][3], w2f[7][3], hf[7][0]);
  SCHED();
  MF16A(a2[1][0], w2f[7][0], hf[7][1]);
  ldc128<0>(hf[12][1], rw, l16, hq0[1] + 12288);
  MF16A(a2[1][1], w2f[7][1], hf[7][1]);
  MF16A(a2[1][2], w2f[7][2], hf[7][1]);
  asm volatile("s_waitcnt vmcnt(12)" :  :: "memory");
  MF16A(a2[1][3], w2f[7][3], hf[7][1]);
  SCHED();
  MF16A(a2[2][0], w2f[7][0], hf[7][2]);
  ldc128<0>(hf[12][2], rw, l16, hq0[2] + 12288);
  MF16A(a2[2][1], w2f[7][1], hf[7][2]);
  MF16A(a2[2][2], w2f[7][2], hf[7][2]);
  asm volatile("s_waitcnt vmcnt(12)" :  :: "memory");
  MF16A(a2[2][3], w2f[7][3], hf[7][2]);
  SCHED();
  MF16A(a2[3][0], w2f[7][0], hf[7][3]);
  ldc128<0>(hf[12][3], rw, l16, hq0[3] + 12288);
  MF16A(a2[3][1], w2f[7][1], hf[7][3]);
  MF16A(a2[3][2], w2f[7][2], hf[7][3]);
  asm volatile("s_waitcnt vmcnt(12)" :  :: "memory");
  MF16A(a2[3][3], w2f[7][3], hf[7][3]);
  SCHED();
  MF16A(a2[0][0], w2f[8][0], hf[8][0]);
  MF16A(a2[0][1], w2f[8][1], hf[8][0]);
  MF16A(a2[0][2], w2f[8][2], hf[8][0]);
  asm volatile("s_waitcnt vmcnt(11)" :  :: "memory");
  MF16A(a2[0][3], w2f[8][3], hf[8][0]);
  SCHED();
  MF16A(a2[1][0], w2f[8][0], hf[8][1]);
  MF16A(a2[1][1], w2f[8][1], hf[8][1]);
  MF16A(a2[1][2], w2f[8][2], hf[8][1]);
  asm volatile("s_waitcnt vmcnt(10)" :  :: "memory");
  MF16A(a2[1][3], w2f[8][3], hf[8][1]);
  SCHED();
  asm volatile("s_waitcnt vmcnt(7)" : "+v"(fh) :: "memory");
  if (__builtin_expect(!all_eq(fh, tag), 0)) poll(rw, fo, flw + 1 * FLAG_ARR, tag);
  ldc128<2048>(hf[2][0], rw, l16, hq0[0] + 0);
  ldc128<3072>(hf[3][0], rw, l16, hq0[0] + 0);
  SCHED();
  MF16A(a2[2][0], w2f[8][0], hf[8][2]);
  ldc128<2048>(hf[6][0], rw, l16, hq0[0] + 4096);
  MF16A(a2[2][1], w2f[8][1], hf[8][2]);
  MF16A(a2[2][2], w2f[8][2], hf[8][2]);
  MF16A(a2[2][3], w2f[8][3], hf[8][2]);
  SCHED();
  MF16A(a2[3][0], w2f[8][0], hf[8][3]);
  ldc128<1024>(hf[9][0], rw, l16, hq0[0] + 8192);
  MF16A(a2[3][1], w2f[8][1], hf[8][3]);
  MF16A(a2[3][2], w2f[8][2], hf[8][3]);
  MF16A(a2[3][3], w2f[8][3], hf[8][3]);
  SCHED();
  MF16A(a2[0][0], w2f[11][0], hf[11][0]);
  ldc128<2048>(hf[10][0], rw, l16, hq0[0] + 8192);
  MF16A(a2[0][1], w2f[11][1], hf[11][0]);
  MF16A(a2[0][2], w2f[11][2], hf[11][0]);
  asm volatile("s_waitcnt vmcnt(11)" :  :: "memory");
  MF16A(a2[0][3], w2f[11][3], hf[11][0]);
  SCHED();
  MF16A(a2[1][0], w2f[11][0], hf[11][1]);
  ldc128<1024>(hf[13][0], rw, l16, hq0[0] + 12288);
  MF16A(a2[1][1], w2f[11][1], hf[11][1]);
  MF16A(a2[1][2], w2f[11][2], hf[11][1]);
  asm volatile("s_waitcnt vmcnt(11)" :  :: "memory");
  MF16A(a2[1][3], w2f[11][3], hf[11][1]);
  SCHED();
  MF16A(a2[2][0], w2f[11][0], hf[11][2]);
  ldc128<2048>(hf[2][1], rw, l16, hq0[1] + 0);
  MF16A(a2[2][1], w2f[11][1], hf[11][2]);
  MF16A(a2[2][2], w2f[11][2], hf[11][2]);
  asm volatile("s_waitcnt vmcnt(11)" :  :: "memory");
  MF16A(a2[2][3], w2f[11][3], hf[11][2]);
  SCHED();
  MF16A(a2[3][0], w2f[11][0], hf[11][3]);
  ldc128<3072>(hf[3][1], rw, l16, hq0[1] + 0);
  MF16A(a2[3][1], w2f[11][1], hf[11][3]);
  MF16A(a2[3][2], w2f[11][2], hf[11][3]);
  asm volatile("s_waitcnt vmcnt(11)" :  :: "memory");
  MF16A(a2[3][3], w2f[11][3], hf[11][3]);
  SCHED();
  MF16A(a2[0][0], w2f[12][0], hf[12][0]);
  ldc128<2048>(hf[6][1], rw, l16, hq0[1] + 4096);
  MF16A(a2[0][1], w2f[12][1], hf[12][0]);
  MF16A(a2[0][2], w2f[12][2], hf[12][0]);
  asm volatile("s_waitcnt vmcnt(11)" :  :: "memory");
  MF16A(a2[0][3], w2f[12][3], hf[12][0]);
  SCHED();
  MF16A(a2[1][0], w2f[12][0], hf[12][1]);
  ldc128<1024>(hf[9][1], rw, l16, hq0[1] + 8192);
  MF16A(a2[1][1], w2f[12][1], hf[12][1]);
  MF16A(a2[1][2], w2f[12][2], hf[12][1]);
  asm volatile("s_waitcnt vmcnt(11)" :  :: "memory");
  MF16A(a2[1][3], w2f[12][3], hf[12][1]);
  SCHED();
  MF16A(a2[2][0], w2f[12][0], hf[12][2]);
  ldc128<2048>(hf[10][1], rw, l16, hq0[1] + 8192);
  MF16A(a2[2][1], w2f[12][1], hf[12][2]);
  MF16A(a2[2][2], w2f[12][2], hf[12][2]);
  asm volatile("s_waitcnt vmcnt(11)" :  :: "memory");
  MF16A(a2[2][3], w2f[12][3], hf[12][2]);
  SCHED();
  MF16A(a2[3][0], w2f[12][0], hf[12][3]);
  ldc128<1024>(hf[13][1], rw, l16, hq0[1] + 12288);
  MF16A(a2[3][1], w2f[12][1], hf[12][3]);
  MF16A(a2[3][2], w2f[12][2], hf[12][3]);
  asm volatile("s_waitcnt vmcnt(11)" :  :: "memory");
  MF16A(a2[3][3], w2f[12][3], hf[12][3]);
  SCHED();
  MF16A(a2[0][0], w2f[2][0], hf[2][0]);
  ldc128<2048>(hf[2][2], rw, l16, hq0[2] + 0);
  MF16A(a2[0][1], w2f[2][1], hf[2][0]);
  MF16A(a2[0][2], w2f[2][2], hf[2][0]);
  asm volatile("s_waitcnt vmcnt(11)" :  :: "memory");
  MF16A(a2[0][3], w2f[2][3], hf[2][0]);
  SCHED();
  MF16A(a2[0][0], w2f[3][0], hf[3][0]);
  ldc128<3072>(hf[3][2], rw, l16, hq0[2] + 0);
  MF16A(a2[0][1], w2f[3][1], hf[3][0]);
  MF16A(a2[0][2], w2f[3][2], hf[3][0]);
  asm volatile("s_waitcnt vmcnt(11)" :  :: "memory");
  MF16A(a2[0][3], w2f[3][3], hf[3][0]);
  SCHED();
  MF16A(a2[0][0], w2f[6][0], hf[6][0]);
  ldc128<2048>(hf[6][2], rw, l16, hq0[2] + 4096);
  MF16A(a2[0][1], w2f[6][1], hf[6][0]);
  MF16A(a2[0][2], w2f[6][2], hf[6][0]);
  asm volatile("s_waitcnt vmcnt(11)" :  :: "memory");
  MF16A(a2[0][3], w2f[6][3], hf[6][0]);
  SCHED();
  MF16A(a2[0][0], w2f[9][0], hf[9][0]);
  ldc128<1024>(hf[9][2], rw, l16, hq0[2] + 8192);
  MF16A(a2[0][1], w2f[9][1], hf[9][0]);
  MF16A(a2[0][2], w2f[9][2], hf[9][0]);
  asm volatile("s_waitcnt vmcnt(11)" :  :: "memory");
  MF16A(a2[0][3], w2f[9][3], hf[9][0]);
  SCHED();
  MF16A(a2[0][0], w2f[10][0], hf[10][0]);
  ldc128<2048>(hf[10][2], rw, l16, hq0[2] + 8192);
  MF16A(a2[0][1], w2f[10][1], hf[10][0]);
  MF16A(a2[0][2], w2f[10][2], hf[10][0]);
  asm volatile("s_waitcnt vmcnt(11)" :  :: "memory");
  MF16A(a2[0][3], w2f[10][3], hf[10][0]);
  SCHED();
  MF16A(a2[0][0], w2f[13][0], hf[13][0]);
  ldc128<1024>(hf[13][2], rw, l16, hq0[2] + 12288);
  MF16A(a2[0][1], w2f[13][1], hf[13][0]);
  MF16A(a2[0][2], w2f[13][2], hf[13][0]);
  asm volatile("s_waitcnt vmcnt(11)" :  :: "memory");
  MF16A(a2[0][3], w2f[13][3], hf[13][0]);
  SCHED();
  MF16A(a2[1][0], w2f[2][0], hf[2][1]);
  ldc128<2048>(hf[2][3], rw, l16, hq0[3] + 0);
  MF16A(a2[1][1], w2f[2][1], hf[2][1]);
  MF16A(a2[1][2], w2f[2][2], hf[2][1]);
  asm volatile("s_waitcnt vmcnt(11)" :  :: "memory");
  MF16A(a2[1][3], w2f[2][3], hf[2][1]);
  SCHED();
  MF16A(a2[1][0], w2f[3][0], hf[3][1]);
  ldc128<3072>(hf[3][3], rw, l16, hq0[3] + 0);
  MF16A(a2[1][1], w2f[3][1], hf[3][1]);
  SCHED(); *(LAS f32x4*)(L + ro[0] + 0) = a2[0][0]; SCHED();
  MF16A(a2[1][2], w2f[3][2], hf[3][1]);
  asm volatile("s_waitcnt vmcnt(11)" :  :: "memory");
  MF16A(a2[1][3], w2f[3][3], hf[3][1]);
  SCHED();
  MF16A(a2[1][0], w2f[6][0], hf[6][1]);
  ldc128<2048>(hf[6][3], rw, l16, hq0[3] + 4096);
  SCHED(); *(LAS f32x4*)(L + ro[0] + 1024) = a2[0][1]; SCHED();
  MF16A(a2[1][1], w2f[6][1], hf[6][1]);
  MF16A(a2[1][2], w2f[6][2], hf[6][1]);
  asm volatile("s_waitcnt vmcnt(11)" :  :: "memory");
  MF16A(a2[1][3], w2f[6][3], hf[6][1]);
  SCHED(); *(LAS f32x4*)(L + ro[0] + 2048) = a2[0][2]; SCHED();
  SCHED();
  MF16A(a2[1][0], w2f[9][0], hf[9][1]);
  ldc128<1024>(hf[9][3], rw, l16, hq0[3] + 8192);
  MF16A(a2[1][1], w2f[9][1], hf[9][1]);
  MF16A(a2[1][2], w2f[9][2], hf[9][1]);
  asm volatile("s_waitcnt vmcnt(11)" :  :: "memory");
  SCHED(); *(LAS f32x4*)(L + ro[0] + 3072) = a2[0][3]; SCHED();
  MF16A(a2[1][3], w2f[9][3], hf[9][1]);
  SCHED();
  MF16A(a2[1][0], w2f[10][0], hf[10][1]);
  ldc128<2048>(hf[10][3], rw, l16, hq0[3] + 8192);
  MF16A(a2[1][1], w2f[10][1], hf[10][1]);
  MF16A(a2[1][2], w2f[10][2], hf[10][1]);
  asm volatile("s_waitcnt vmcnt(11)" :  :: "memory");
  MF16A(a2[1][3], w2f[10][3], hf[10][1]);
  SCHED();
  MF16A(a2[1][0], w2f[13][0], hf[13][1]);
  ldc128<1024>(hf[13][3], rw, l16, hq0[3] + 12288);
  MF16A(a2[1][1], w2f[13][1], hf[13][1]);
  MF16A(a2[1][2], w2f[13][2], hf[13][1]);
  asm volatile("s_waitcnt vmcnt(11)" :  :: "memory");
  MF16A(a2[1][3], w2f[13][3], hf[13][1]);
  SCHED();
  MF16A(a2[2][0], w2f[2][0], hf[2][2]);
  MF16A(a2[2][1], w2f[2][1], hf[2][2]);
  MF16A(a2[2][2], w2f[2][2], hf[2][2]);
  asm volatile("s_waitcnt vmcnt(10)" :  :: "memory");
  MF16A(a2[2][3], w2f[2][3], hf[2][2]);
  SCHED();
  MF16A(a2[2][0], w2f[3][0], hf[3][2]);
  MF16A(a2[2][1], w2f[3][1], hf[3][2]);
  SCHED(); *(LAS f32x4*)(L + ro[1] + 0) = a2[1][0]; SCHED();
  MF16A(a2[2][2], w2f[3][2], hf[3][2]);
  asm volatile("s_waitcnt vmcnt(9)" :  :: "memory");
  MF16A(a2[2][3], w2f[3][3], hf[3][2]);
  SCHED();
  MF16A(a2[2][0], w2f[6][0], hf[6][2]);
  SCHED(); *(LAS f32x4*)(L + ro[1] + 1024) = a2[1][1]; SCHED();
  MF16A(a2[2][1], w2f[6][1], hf[6][2]);
  MF16A(a2[2][2], w2f[6][2], hf[6][2]);
  asm volatile("s_waitcnt vmcnt(8)" :  :: "memory");
  MF16A(a2[2][3], w2f[6][3], hf[6][2]);
  SCHED(); *(LAS f32x4*)(L + ro[1] + 2048) = a2[1][2]; SCHED();
  SCHED();
  MF16A(a2[2][0], w2f[9][0], hf[9][2]);
  MF16A(a2[2][1], w2f[9][1], hf[9][2]);
  MF16A(a2[2][2], w2f[9][2], hf[9][2]);
  asm volatile("s_waitcnt vmcnt(7)" :  :: "memory");
  SCHED(); *(LAS f32x4*)(L + ro[1] + 3072) = a2[1][3]; SCHED();
  MF16A(a2[2][3], w2f[9][3], hf[9][2]);
  SCHED();
  MF16A(a2[2][0], w2f[10][0], hf[10][2]);
  MF16A(a2[2][1], w2f[10][1], hf[10][2]);
  MF16A(a2[2][2], w2f[10][2], hf[10][2]);
  asm volatile("s_waitcnt vmcnt(6)" :  :: "memory");
  MF16A(a2[2][3], w2f[10][3], hf[10][2]);
  SCHED();
  MF16A(a2[2][0], w2f[13][0], hf[13][2]);
  MF16A(a2[2][1], w2f[13][1], hf[13][2]);
  MF16A(a2[2][2], w2f[13][2], hf[13][2]);
  asm volatile("s_waitcnt vmcnt(5)" :  :: "memory");
  MF16A(a2[2][3], w2f[13][3], hf[13][2]);
  SCHED();
  MF16A(a2[3][0], w2f[2][0], hf[2][3]);
  MF16A(a2[3][1], w2f[2][1], hf[2][3]);
  MF16A(a2[3][2], w2f[2][2], hf[2][3]);
  asm volatile("s_waitcnt vmcnt(4)" :  :: "memory");
  MF16A(a2[3][3], w2f[2][3], hf[2][3]);
  SCHED();
  MF16A(a2[3][0], w2f[3][0], hf[3][3]);
  MF16A(a2[3][1], w2f[3][1], hf[3][3]);
  SCHED(); *(LAS f32x4*)(L + ro[2] + 0) = a2[2][0]; SCHED();
  MF16A(a2[3][2], w2f[3][2], hf[3][3]);
  asm volatile("s_waitcnt vmcnt(3)" :  :: "memory");
  MF16A(a2[3][3], w2f[3][3], hf[3][3]);
  SCHED();
  MF16A(a2[3][0], w2f[6][0], hf[6][3]);
  SCHED(); *(LAS f32x4*)(L + ro[2] + 1024) = a2[2][1]; SCHED();
  MF16A(a2[3][1], w2f[6][1], hf[6][3]);
  MF16A(a2[3][2], w2f[6][2], hf[6][3]);
  asm volatile("s_waitcnt vmcnt(2)" :  :: "memory");
  MF16A(a2[3][3], w2f[6][3], hf[6][3]);
  SCHED(); *(LAS f32x4*)(L + ro[2] + 2048) = a2[2][2]; SCHED();
  SCHED();
  MF16A(a2[3][0], w2f[9][0], hf[9][3]);
  MF16A(a2[3][1], w2f[9][1], hf[9][3]);
  MF16A(a2[3][2], w2f[9][2], hf[9][3]);
  asm volatile("s_waitcnt vmcnt(1)" :  :: "memory");
  SCHED(); *(LAS f32x4*)(L + ro[2] + 3072) = a2[2][3]; SCHED();
  MF16A(a2[3][3], w2f[9][3], hf[9][3]);
  SCHED();
  MF16A(a2[3][0], w2f[10][0], hf[10][3]);
  BAR();
  SCHED(); rin[0] = *(LAS f32x4*)(L + ri + 0); SCHED();
  SCHED(); rin[1] = *(LAS f32x4*)(L + ri + 1024); SCHED();
  MF16A(a2[3][1], w2f[10][1], hf[10][3]);
  SCHED(); rin[2] = *(LAS f32x4*)(L + ri + 2048); SCHED();
  SCHED(); rin[3] = *(LAS f32x4*)(L + ri + 3072); SCHED();
  MF16A(a2[3][2], w2f[10][2], hf[10][3]);
  asm volatile("s_waitcnt vmcnt(0)" :  :: "memory");
  SCHED(); rin[4] = *(LAS f32x4*)(L + ri + 4096); SCHED();
  SCHED(); rin[5] = *(LAS f32x4*)(L + ri + 5120); SCHED();
  MF16A(a2[3][3], w2f[10][3], hf[10][3]);
  SCHED(); rin[6] = *(LAS f32x4*)(L + ri + 6144); SCHED();
  SCHED(); rin[7] = *(LAS f32x4*)(L + ri + 7168); SCHED();
  SCHED();
  MF16A(a2[3][0], w2f[13][0], hf[13][3]);
  SCHED(); rin[8] = *(LAS f32x4*)(L + ri + 8192); SCHED();
  SCHED(); rin[9] = *(LAS f32x4*)(L + ri + 9216); SCHED();
  MF16A(a2[3][1], w2f[13][1], hf[13][3]);
  SCHED(); rin[10] = *(LAS f32x4*)(L + ri + 10240); SCHED();
  SCHED(); rin[11] = *(LAS f32x4*)(L + ri + 11264); SCHED();
  MF16A(a2[3][2], w2f[13][2], hf[13][3]);
  MF16A(a2[3][3], w2f[13][3], hf[13][3]);
  SCHED();
  NOPS(7);
  finish16(rin, a2[3], xr);
  row_sums(xr, rs1, rs2);
  if (q4 == 0) st128(v4u{f2u(rs1), tag32, f2u(rs2), tag32}, rw, l16, rso);
  PIN(yp[0]); PIN(yp[1]); unpack16(xr, xq); unpack16(yp, yq);
  __builtin_amdgcn_s_sleep(10);
  ldc128<0>(gs[0], rw, l16, rsg);
  ldc128<1024>(gs[1], rw, l16, rsg);
  asm volatile("s_waitcnt vmcnt(0)" : "+v"(gs[0]), "+v"(gs[1]) :: "memory");
  gather_fix(gs, rw, l16, rsg, tag32);
  { float mu, r; ln1_consts(gs, mu, r);
    gate_y(xr[0], xq + 0, yq + 0, mu, r);
    st128(xr[0], rw, l16 + 0, xo3); SCHED();
    gate_y(xr[1], xq + 4, yq + 4, mu, r);
    st128(xr[1], rw, l16 + 1024, xo3); SCHED();
  }
  row_sums(xr, rs1, rs2);
  asm volatile("s_waitcnt vmcnt(0)" :  :: "memory");
  if (q4 == 0) st128(v4u{f2u(rs1), tag32, f2u(rs2), tag32}, rw, l16, ro3);
  __builtin_amdgcn_s_sleep(12);
  ldc128<0>(ga[0], rw, l16, rg1);
  ldc128<1024>(ga[1], rw, l16, rg1);
  ldc128<2048>(ga[2], rw, l16, rg1);
  ldc128<3072>(ga[3], rw, l16, rg1);
  asm volatile("s_waitcnt vmcnt(0)" : "+v"(ga[0]), "+v"(ga[1]), "+v"(ga[2]), "+v"(ga[3]) :: "memory");
  gather_fix(ga, rw, l16, rg1, tag32);
  ldc128<0>(xf[0][0], rw, l16, xb1 + 0);
  ldc128<0>(xf[1][0], rw, l16, xb1 + XT + 0);
  ldc128<1024>(xf[0][1], rw, l16, xb1 + 0);
  ldc128<1024>(xf[1][1], rw, l16, xb1 + XT + 0);
  ldc128<2048>(xf[0][2], rw, l16, xb1 + 0);
  ldc128<2048>(xf[1][2], rw, l16, xb1 + XT + 0);
  ldc128<3072>(xf[0][3], rw, l16, xb1 + 0);
  ldc128<3072>(xf[1][3], rw, l16, xb1 + XT + 0);
  ldc128<0>(xf[0][4], rw, l16, xb1 + 4096);
  ldc128<0>(xf[1][4], rw, l16, xb1 + XT + 4096);
  al[0] = trd(L + wa0 + 0); ah[0] = trd(L + wa0 + 3584);
  al[1] = trd(L + wa0 + 32); ah[1] = trd(L + wa0 + 3616);
  al[2] = trd(L + wa0 + 64); ah[2] = trd(L + wa0 + 3648);
  al[3] = trd(L + wa0 + 96); ah[3] = trd(L + wa0 + 3680);
  SCHED();
  ln2_consts(ga, q4, gc, gd, mh, sh);
  SCHED();
  al[4] = trd(L + wa0 + 7168); SCHED();
  asm volatile("s_waitcnt vmcnt(9)" :  :: "memory");
  { const v4u a = {al[0].x, al[0].y, ah[0].x, ah[0].y}; MF16Z(a1[0][0], a, xf[0][0]); SCHED();
    ah[4] = trd(L + wa0 + 10752); SCHED();
  asm volatile("s_waitcnt vmcnt(8)" :  :: "memory");
    MF16Z(a1[1][0], a, xf[1][0]); } SCHED();
  al[0] = trd(L + wa0 + 7200); SCHED();
  { const v4u a = {al[1].x, al[1].y, ah[1].x, ah[1].y}; MF16Z(a1[0][1], a, xf[0][0]); SCHED();
    ah[0] = trd(L + wa0 + 10784); SCHED();
    MF16Z(a1[1][1], a, xf[1][0]); } SCHED();
  ldc128<1024>(xf[0][5], rw, l16, xb1 + 4096);
  SCHED();
  al[1] = trd(L + wa0 + 7232); SCHED();
  { const v4u a = {al[2].x, al[2].y, ah[2].x, ah[2].y}; MF16Z(a1[0][2], a, xf[0][0]); SCHED();
    ah[1] = trd(L + wa0 + 10816); SCHED();
    MF16Z(a1[1][2], a, xf[1][0]); } SCHED();
  al[2] = trd(L + wa0 + 7264); SCHED();
  { const v4u a = {al[3].x, al[3].y, ah[3].x, ah[3].y}; MF16Z(a1[0][3], a, xf[0][0]); SCHED();
    ah[2] = trd(L + wa0 + 10848); SCHED();
    MF16Z(a1[1][3], a, xf[1][0]); } SCHED();
  ldc128<1024>(xf[1][5], rw, l16, xb1 + XT + 4096);
  SCHED();
  al[3] = trd(L + wa0 + 14336); SCHED();
  asm volatile("s_waitcnt vmcnt(9)" :  :: "memory");
  { const v4u a = {al[4].x, al[4].y, ah[4].x, ah[4].y}; MF16(a1[0][0], a, xf[0][1]); SCHED();
    ah[3] = trd(L + wa0 + 17920); SCHED();
  asm volatile("s_waitcnt vmcnt(8)" :  :: "memory");
    MF16(a1[1][0], a, xf[1][1]); } SCHED();
  al[4] = trd(L + wa0 + 14368); SCHED();
  { const v4u a = {al[0].x, al[0].y, ah[0].x, ah[0].y}; MF16(a1[0][1], a, xf[0][1]); SCHED();
    ah[4] = trd(L + wa0 + 17952); SCHED();
    MF16(a1[1][1], a, xf[1][1]); } SCHED();
  ldc128<2048>(xf[0][6], rw, l16, xb1 + 4096);
  SCHED();
  al[0] = trd(L + wa0 + 14400); SCHED();
  { const v4u a = {al[1].x, al[1].y, ah[1].x, ah[1].y}; MF16(a1[0][2], a, xf[0][1]); SCHED();
    ah[0] = trd(L + wa0 + 17984); SCHED();
    MF16(a1[1][2], a, xf[1][1]); } SCHED();
  al[1] = trd(L + wa0 + 14432); SCHED();
  { const v4u a = {al[2].x, al[2].y, ah[2].x, ah[2].y}; MF16(a1[0][3], a, xf[0][1]); SCHED();
    ah[1] = trd(L + wa0 + 18016); SCHED();
    MF16(a1[1][3], a, xf[1][1]); } SCHED();
  ldc128<2048>(xf[1][6], rw, l16, xb1 + XT + 4096);
  SCHED();
  al[2] = trd(L + wa0 + 21504); SCHED();
  asm volatile("s_waitcnt vmcnt(9)" :  :: "memory");
  { const v4u a = {al[3].x, al[3].y, ah[3].x, ah[3].y}; MF16(a1[0][0], a, xf[0][2]); SCHED();
    ah[2] = trd(L + wa0 + 25088); SCHED();
  asm volatile("s_waitcnt vmcnt(8)" :  :: "memory");
    MF16(a1[1][0], a, xf[1][2]); } SCHED();
  al[3] = trd(L + wa0 + 21536); SCHED();
  { const v4u a = {al[4].x, al[4].y, ah[4].x, ah[4].y}; MF16(a1[0][1], a, xf[0][2]); SCHED();
    ah[3] = trd(L + wa0 + 25120); SCHED();
    MF16(a1[1][1], a, xf[1][2]); } SCHED();
  ldc128<3072>(xf[0][7], rw, l16, xb1 + 4096);
  SCHED();
  al[4] = trd(L + wa0 + 21568); SCHED();
  { const v4u a = {al[0].x, al[0].y, ah[0].x, ah[0].y}; MF16(a1[0][2], a, xf[0][2]); SCHED();
    ah[4] = trd(L + wa0 + 25152); SCHED();
    MF16(a1[1][2], a, xf[1][2]); } SCHED();
  al[0] = trd(L + wa0 + 21600); SCHED();
  { const v4u a = {al[1].x, al[1].y, ah[1].x, ah[1].y}; MF16(a1[0][3], a, xf[0][2]); SCHED();
    ah[0] = trd(L + wa0 + 25184); SCHED();
    MF16(a1[1][3], a, xf[1][2]); } SCHED();
  ldc128<3072>(xf[1][7], rw, l16, xb1 + XT + 4096);
  SCHED();
  al[1] = trd(L + wa0 + 28672); SCHED();
  asm volatile("s_waitcnt vmcnt(9)" :  :: "memory");
  { const v4u a = {al[2].x, al[2].y, ah[2].x, ah[2].y}; MF16(a1[0][0], a, xf[0][3]); SCHED();
    ah[1] = trd(L + wa0 + 32256); SCHED();
  asm volatile("s_waitcnt vmcnt(8)" :  :: "memory");
    MF16(a1[1][0], a, xf[1][3]); } SCHED();
  al[2] = trd(L + wa0 + 28704); SCHED();
  { const v4u a = {al[3].x, al[3].y, ah[3].x, ah[3].y}; MF16(a1[0][1], a, xf[0][3]); SCHED();
    ah[2] = trd(L + wa0 + 32288); SCHED();
    MF16(a1[1][1], a, xf[1][3]); } SCHED();
  ldc128<0>(xf[0][8], rw, l16, xb1 + 8192);
  SCHED();
  al[3] = trd(L + wa0 + 28736); SCHED();
  { const v4u a = {al[4].x, al[4].y, ah[4].x, ah[4].y}; MF16(a1[0][2], a, xf[0][3]); SCHED();
    ah[3] = trd(L + wa0 + 32320); SCHED();
    MF16(a1[1][2], a, xf[1][3]); } SCHED();
  al[4] = trd(L + wa0 + 28768); SCHED();
  { const v4u a = {al[0].x, al[0].y, ah[0].x, ah[0].y}; MF16(a1[0][3], a, xf[0][3]); SCHED();
    ah[4] = trd(L + wa0 + 32352); SCHED();
    MF16(a1[1][3], a, xf[1][3]); } SCHED();
  ldc128<0>(xf[1][8], rw, l16, xb1 + XT + 8192);
  SCHED();
  al[0] = trd(L + wa0 + 35840); SCHED();
  asm volatile("s_waitcnt vmcnt(9)" :  :: "memory");
  { const v4u a = {al[1].x, al[1].y, ah[1].x, ah[1].y}; MF16(a1[0][0], a, xf[0][4]); SCHED();
    ah[0] = trd(L + wa0 + 39424); SCHED();
  asm volatile("s_waitcnt vmcnt(8)" :  :: "memory");
    MF16(a1[1][0], a, xf[1][4]); } SCHED();
  al[1] = trd(L + wa0 + 35872); SCHED();
  { const v4u a = {al[2].x, al[2].y, ah[2].x, ah[2].y}; MF16(a1[0][1], a, xf[0][4]); SCHED();
    ah[1] = trd(L + wa0 + 39456); SCHED();
    MF16(a1[1][1], a, xf[1][4]); } SCHED();
  ldc128<1024>(xf[0][9], rw, l16, xb1 + 8192);
  SCHED();
  al[2] = trd(L + wa0 + 35904); SCHED();
  { const v4u a = {al[3].x, al[3].y, ah[3].x, ah[3].y}; MF16(a1[0][2], a, xf[0][4]); SCHED();
    ah[2] = trd(L + wa0 + 39488); SCHED();
    MF16(a1[1][2], a, xf[1][4]); } SCHED();
  al[3] = trd(L + wa0 + 35936); SCHED();
  { const v4u a = {al[4].x, al[4].y, ah[4].x, ah[4].y}; MF16(a1[0][3], a, xf[0][4]); SCHED();
    ah[3] = trd(L + wa0 + 39520); SCHED();
    MF16(a1[1][3], a, xf[1][4]); } SCHED();
  ldc128<1024>(xf[1][9], rw, l16, xb1 + XT + 8192);
  SCHED();
  al[4] = trd(L + wa0 + 43008); SCHED();
  asm volatile("s_waitcnt vmcnt(9)" :  :: "memory");
  { const v4u a = {al[0].x, al[0].y, ah[0].x, ah[0].y}; MF16(a1[0][0], a, xf[0][5]); SCHED();
    ah[4] = trd(L + wa0 + 46592); SCHED();
  asm volatile("s_waitcnt vmcnt(8)" :  :: "memory");
    MF16(a1[1][0], a, xf[1][5]); } SCHED();
  al[0] = trd(L + wa0 + 43040); SCHED();
  { const v4u a = {al[1].x, al[1].y, ah[1].x, ah[1].y}; MF16(a1[0][1], a, xf[0][5]); SCHED();
    ah[0] = trd(L + wa0 + 46624); SCHED();
    MF16(a1[1][1], a, xf[1][5]); } SCHED();
  ldc128<2048>(xf[0][10], rw, l16, xb1 + 8192);
  SCHED();
  al[1] = trd(L + wa0 + 43072); SCHED();
  { const v4u a = {al[2].x, al[2].y, ah[2].x, ah[2].y}; MF16(a1[0][2], a, xf[0][5]); SCHED();
    ah[1] = trd(L + wa0 + 46656); SCHED();
    MF16(a1[1][2], a, xf[1][5]); } SCHED();
  al[2] = trd(L + wa0 + 43104); SCHED();
  { const v4u a = {al[3].x, al[3].y, ah[3].x, ah[3].y}; MF16(a1[0][3], a, xf[0][5]); SCHED();
    ah[2] = trd(L + wa0 + 46688); SCHED();
    MF16(a1[1][3], a, xf[1][5]); } SCHED();
  ldc128<2048>(xf[1][10], rw, l16, xb1 + XT + 8192);
  SCHED();
  al[3] = trd(L + wa0 + 50176); SCHED();
  asm volatile("s_waitcnt vmcnt(9)" :  :: "memory");
  { const v4u a = {al[4].x, al[4].y, ah[4].x, ah[4].y}; MF16(a1[0][0], a, xf[0][6]); SCHED();
    ah[3] = trd(L + wa0 + 53760); SCHED();
  asm volatile("s_waitcnt vmcnt(8)" :  :: "memory");
    MF16(a1[1][0], a, xf[1][6]); } SCHED();
  al[4] = trd(L + wa0 + 50208); SCHED();
  { const v4u a = {al[0].x, al[0].y, ah[0].x, ah[0].y}; MF16(a1[0][1], a, xf[0][6]); SCHED();
    ah[4] = trd(L + wa0 + 53792); SCHED();
    MF16(a1[1][1], a, xf[1][6]); } SCHED();
  ldc128<3072>(xf[0][11], rw, l16, xb1 + 8192);
  SCHED();
  al[0] = trd(L + wa0 + 50240); SCHED();
  { const v4u a = {al[1].x, al[1].y, ah[1].x, ah[1].y}; MF16(a1[0][2], a, xf[0][6]); SCHED();
    ah[0] = trd(L + wa0 + 53824); SCHED();
    MF16(a1[1][2], a, xf[1][6]); } SCHED();
  al[1] = trd(L + wa0 + 50272); SCHED();
  { const v4u a = {al[2].x, al[2].y, ah[2].x, ah[2].y}; MF16(a1[0][3], a, xf[0][6]); SCHED();
    ah[1] = trd(L + wa0 + 53856); SCHED();
    MF16(a1[1][3], a, xf[1][6]); } SCHED();
  ldc128<3072>(xf[1][11], rw, l16, xb1 + XT + 8192);
  SCHED();
  al[2] = trd(L + wa1 + 0); SCHED();
  asm volatile("s_waitcnt vmcnt(9)" :  :: "memory");
  { const v4u a = {al[3].x, al[3].y, ah[3].x, ah[3].y}; MF16(a1[0][0], a, xf[0][7]); SCHED();
    ah[2] = trd(L + wa1 + 3584); SCHED();
  asm volatile("s_waitcnt vmcnt(8)" :  :: "memory");
    MF16(a1[1][0], a, xf[1][7]); } SCHED();
  al[3] = trd(L + wa1 + 32); SCHED();
  { const v4u a = {al[4].x, al[4].y, ah[4].x, ah[4].y}; MF16(a1[0][1], a, xf[0][7]); SCHED();
    ah[3] = trd(L + wa1 + 3616); SCHED();
    MF16(a1[1][1], a, xf[1][7]); } SCHED();
  ldc128<0>(xf[0][12], rw, l16, xb1 + 12288);
  SCHED();
  al[4] = trd(L + wa1 + 64); SCHED();
  { const v4u a = {al[0].x, al[0].y, ah[0].x, ah[0].y}; MF16(a1[0][2], a, xf[0][7]); SCHED();
    ah[4] = trd(L + wa1 + 3648); SCHED();
    MF16(a1[1][2], a, xf[1][7]); } SCHED();
  al[0] = trd(L + wa1 + 96); SCHED();
  { const v4u a = {al[1].x, al[1].y, ah[1].x, ah[1].y}; MF16(a1[0][3], a, xf[0][7]); SCHED();
    ah[0] = trd(L + wa1 + 3680); SCHED();
    MF16(a1[1][3], a, xf[1][7]); } SCHED();
  ldc128<0>(xf[1][12], rw, l16, xb1 + XT + 12288);
  SCHED();
  al[1] = trd(L + wa1 + 7168); SCHED();
  asm volatile("s_waitcnt vmcnt(9)" :  :: "memory");
  { const v4u a = {al[2].x, al[2].y, ah[2].x, ah[2].y}; MF16(a1[0][0], a, xf[0][8]); SCHED();
    ah[1] = trd(L + wa1 + 10752); SCHED();
  asm volatile("s_waitcnt vmcnt(8)" :  :: "memory");
    MF16(a1[1][0], a, xf[1][8]); } SCHED();
  al[2] = trd(L + wa1 + 7200); SCHED();
  { const v4u a = {al[3].x, al[3].y, ah[3].x, ah[3].y}; MF16(a1[0][1], a, xf[0][8]); SCHED();
    ah[2] = trd(L + wa1 + 10784); SCHED();
    MF16(a1[1][1], a, xf[1][8]); } SCHED();
  ldc128<1024>(xf[0][13], rw, l16, xb1 + 12288);
  SCHED();
  al[3] = trd(L + wa1 + 7232); SCHED();
  { const v4u a = {al[4].x, al[4].y, ah[4].x, ah[4].y}; MF16(a1[0][2], a, xf[0][8]); SCHED();
    ah[3] = trd(L + wa1 + 10816); SCHED();
    MF16(a1[1][2], a, xf[1][8]); } SCHED();
  al[4] = trd(L + wa1 + 7264); SCHED();
  { const v4u a = {al[0].x, al[0].y, ah[0].x, ah[0].y}; MF16(a1[0][3], a, xf[0][8]); SCHED();
    ah[4] = trd(L + wa1 + 10848); SCHED();
    MF16(a1[1][3], a, xf[1][8]); } SCHED();
  ldc128<1024>(xf[1][13], rw, l16, xb1 + XT + 12288);
  SCHED();
  al[0] = trd(L + wa1 + 14336); SCHED();
  asm volatile("s_waitcnt vmcnt(9)" :  :: "memory");
  { const v4u a = {al[1].x, al[1].y, ah[1].x, ah[1].y}; MF16(a1[0][0], a, xf[0][9]); SCHED();
    ah[0] = trd(L + wa1 + 17920); SCHED();
  asm volatile("s_waitcnt vmcnt(8)" :  :: "memory");
    MF16(a1[1][0], a, xf[1][9]); } SCHED();
  al[1] = trd(L + wa1 + 14368); SCHED();
  { const v4u a = {al[2].x, al[2].y, ah[2].x, ah[2].y}; MF16(a1[0][1], a, xf[0][9]); SCHED();
    ah[1] = trd(L + wa1 + 17952); SCHED();
    MF16(a1[1][1], a, xf[1][9]); } SCHED();
  ldc128<2048>(xf[0][14], rw, l16, xb1 + 12288);
  SCHED();
  al[2] = trd(L + wa1 + 14400); SCHED();
  { const v4u a = {al[3].x, al[3].y, ah[3].x, ah[3].y}; MF16(a1[0][2], a, xf[0][9]); SCHED();
    ah[2] = trd(L + wa1 + 17984); SCHED();
    MF16(a1[1][2], a, xf[1][9]); } SCHED();
  al[3] = trd(L + wa1 + 14432); SCHED();
  { const v4u a = {al[4].x, al[4].y, ah[4].x, ah[4].y}; MF16(a1[0][3], a, xf[0][9]); SCHED();
    ah[3] = trd(L + wa1 + 18016); SCHED();
    MF16(a1[1][3], a, xf[1][9]); } SCHED();
  ldc128<2048>(xf[1][14], rw, l16, xb1 + XT + 12288);
  SCHED();
  al[4] = trd(L + wa1 + 21504); SCHED();
  asm volatile("s_waitcnt vmcnt(9)" :  :: "memory");
  { const v4u a = {al[0].x, al[0].y, ah[0].x, ah[0].y}; MF16(a1[0][0], a, xf[0][10]); SCHED();
    ah[4] = trd(L + wa1 + 25088); SCHED();
  asm volatile("s_waitcnt vmcnt(8)" :  :: "memory");
    MF16(a1[1][0], a, xf[1][10]); } SCHED();
  al[0] = trd(L + wa1 + 21536); SCHED();
  { const v4u a = {al[1].x, al[1].y, ah[1].x, ah[1].y}; MF16(a1[0][1], a, xf[0][10]); SCHED();
    ah[0] = trd(L + wa1 + 25120); SCHED();
    MF16(a1[1][1], a, xf[1][10]); } SCHED();
  ldc128<3072>(xf[0][15], rw, l16, xb1 + 12288);
  SCHED();
  al[1] = trd(L + wa1 + 21568); SCHED();
  { const v4u a = {al[2].x, al[2].y, ah[2].x, ah[2].y}; MF16(a1[0][2], a, xf[0][10]); SCHED();
    ah[1] = trd(L + wa1 + 25152); SCHED();
    MF16(a1[1][2], a, xf[1][10]); } SCHED();
  al[2] = trd(L + wa1 + 21600); SCHED();
  { const v4u a = {al[3].x, al[3].y, ah[3].x, ah[3].y}; MF16(a1[0][3], a, xf[0][10]); SCHED();
    ah[2] = trd(L + wa1 + 25184); SCHED();
    MF16(a1[1][3], a, xf[1][10]); } SCHED();
  ldc128<3072>(xf[1][15], rw, l16, xb1 + XT + 12288);
  SCHED();
  al[3] = trd(L + wa1 + 28672); SCHED();
  asm volatile("s_waitcnt vmcnt(9)" :  :: "memory");
  { const v4u a = {al[4].x, al[4].y, ah[4].x, ah[4].y}; MF16(a1[0][0], a, xf[0][11]); SCHED();
    ah[3] = trd(L + wa1 + 32256); SCHED();
  asm volatile("s_waitcnt vmcnt(8)" :  :: "memory");
    MF16(a1[1][0], a, xf[1][11]); } SCHED();
  al[4] = trd(L + wa1 + 28704); SCHED();
  { const v4u a = {al[0].x, al[0].y, ah[0].x, ah[0].y}; MF16(a1[0][1], a, xf[0][11]); SCHED();
    ah[4] = trd(L + wa1 + 32288); SCHED();
    MF16(a1[1][1], a, xf[1][11]); } SCHED();
  al[0] = trd(L + wa1 + 28736); SCHED();
  { const v4u a = {al[1].x, al[1].y, ah[1].x, ah[1].y}; MF16(a1[0][2], a, xf[0][11]); SCHED();
    ah[0] = trd(L + wa1 + 32320); SCHED();
    MF16(a1[1][2], a, xf[1][11]); } SCHED();
  al[1] = trd(L + wa1 + 28768); SCHED();
  { const v4u a = {al[2].x, al[2].y, ah[2].x, ah[2].y}; MF16(a1[0][3], a, xf[0][11]); SCHED();
    ah[1] = trd(L + wa1 + 32352); SCHED();
    MF16(a1[1][3], a, xf[1][11]); } SCHED();
  al[2] = trd(L + wa1 + 35840); SCHED();
  asm volatile("s_waitcnt vmcnt(7)" :  :: "memory");
  { const v4u a = {al[3].x, al[3].y, ah[3].x, ah[3].y}; MF16(a1[0][0], a, xf[0][12]); SCHED();
    ah[2] = trd(L + wa1 + 39424); SCHED();
  asm volatile("s_waitcnt vmcnt(6)" :  :: "memory");
    MF16(a1[1][0], a, xf[1][12]); } SCHED();
  al[3] = trd(L + wa1 + 35872); SCHED();
  { const v4u a = {al[4].x, al[4].y, ah[4].x, ah[4].y}; MF16(a1[0][1], a, xf[0][12]); SCHED();
    ah[3] = trd(L + wa1 + 39456); SCHED();
    MF16(a1[1][1], a, xf[1][12]); } SCHED();
  al[4] = trd(L + wa1 + 35904); SCHED();
  { const v4u a = {al[0].x, al[0].y, ah[0].x, ah[0].y}; MF16(a1[0][2], a, xf[0][12]); SCHED();
    ah[4] = trd(L + wa1 + 39488); SCHED();
    MF16(a1[1][2], a, xf[1][12]); } SCHED();
  al[0] = trd(L + wa1 + 35936); SCHED();
  { const v4u a = {al[1].x, al[1].y, ah[1].x, ah[1].y}; MF16(a1[0][3], a, xf[0][12]); SCHED();
    ah[0] = trd(L + wa1 + 39520); SCHED();
    MF16(a1[1][3], a, xf[1][12]); } SCHED();
  al[1] = trd(L + wa1 + 43008); SCHED();
  asm volatile("s_waitcnt vmcnt(5)" :  :: "memory");
  { const v4u a = {al[2].x, al[2].y, ah[2].x, ah[2].y}; MF16(a1[0][0], a, xf[0][13]); SCHED();
    ah[1] = trd(L + wa1 + 46592); SCHED();
  asm volatile("s_waitcnt vmcnt(4)" :  :: "memory");
    MF16(a1[1][0], a, xf[1][13]); } SCHED();
  al[2] = trd(L + wa1 + 43040); SCHED();
  { const v4u a = {al[3].x, al[3].y, ah[3].x, ah[3].y}; MF16(a1[0][1], a, xf[0][13]); SCHED();
    ah[2] = trd(L + wa1 + 46624); SCHED();
    MF16(a1[1][1], a, xf[1][13]); } SCHED();
  al[3] = trd(L + wa1 + 43072); SCHED();
  { const v4u a = {al[4].x, al[4].y, ah[4].x, ah[4].y}; MF16(a1[0][2], a, xf[0][13]); SCHED();
    ah[3] = trd(L + wa1 + 46656); SCHED();
    MF16(a1[1][2], a, xf[1][13]); } SCHED();
  al[4] = trd(L + wa1 + 43104); SCHED();
  { const v4u a = {al[0].x, al[0].y, ah[0].x, ah[0].y}; MF16(a1[0][3], a, xf[0][13]); SCHED();
    ah[4] = trd(L + wa1 + 46688); SCHED();
    MF16(a1[1][3], a, xf[1][13]); } SCHED();
  al[0] = trd(L + wa1 + 50176); SCHED();
  asm volatile("s_waitcnt vmcnt(3)" :  :: "memory");
  { const v4u a = {al[1].x, al[1].y, ah[1].x, ah[1].y}; MF16(a1[0][0], a, xf[0][14]); SCHED();
    ah[0] = trd(L + wa1 + 53760); SCHED();
  asm volatile("s_waitcnt vmcnt(2)" :  :: "memory");
    MF16(a1[1][0], a, xf[1][14]); } SCHED();
  al[1] = trd(L + wa1 + 50208); SCHED();
  { const v4u a = {al[2].x, al[2].y, ah[2].x, ah[2].y}; MF16(a1[0][1], a, xf[0][14]); SCHED();
    ah[1] = trd(L + wa1 + 53792); SCHED();
    MF16(a1[1][1], a, xf[1][14]); } SCHED();
  al[2] = trd(L + wa1 + 50240); SCHED();
  { const v4u a = {al[3].x, al[3].y, ah[3].x, ah[3].y}; MF16(a1[0][2], a, xf[0][14]); SCHED();
    ah[2] = trd(L + wa1 + 53824); SCHED();
    MF16(a1[1][2], a, xf[1][14]); } SCHED();
  al[3] = trd(L + wa1 + 50272); SCHED();
  { const v4u a = {al[4].x, al[4].y, ah[4].x, ah[4].y}; MF16(a1[0][3], a, xf[0][14]); SCHED();
    ah[3] = trd(L + wa1 + 53856); SCHED();
    MF16(a1[1][3], a, xf[1][14]); } SCHED();
  al[4] = trd(L + wa0 + 128); SCHED();
  asm volatile("s_waitcnt vmcnt(1)" :  :: "memory");
  { const v4u a = {al[0].x, al[0].y, ah[0].x, ah[0].y}; MF16(a1[0][0], a, xf[0][15]); SCHED();
    ah[4] = trd(L + wa0 + 3712); SCHED();
  asm volatile("s_waitcnt vmcnt(0)" :  :: "memory");
    MF16(a1[1][0], a, xf[1][15]); } SCHED();
  al[0] = trd(L + wa0 + 160); SCHED();
  { const v4u a = {al[1].x, al[1].y, ah[1].x, ah[1].y}; MF16(a1[0][1], a, xf[0][15]); SCHED();
    ah[0] = trd(L + wa0 + 3744); SCHED();
    MF16(a1[1][1], a, xf[1][15]); } SCHED();
  al[1] = trd(L + wa0 + 192); SCHED();
  { const v4u a = {al[2].x, al[2].y, ah[2].x, ah[2].y}; MF16(a1[0][2], a, xf[0][15]); SCHED();
    ah[1] = trd(L + wa0 + 3776); SCHED();
    MF16(a1[1][2], a, xf[1][15]); } SCHED();
  al[2] = trd(L + wa0 + 7296); SCHED();
  { const v4u a = {al[3].x, al[3].y, ah[3].x, ah[3].y}; MF16(a1[0][3], a, xf[0][15]); SCHED();
    ah[2] = trd(L + wa0 + 10880); SCHED();
    MF16(a1[1][3], a, xf[1][15]); } SCHED();
  { v4u xe0 = {mh[0], mh[0], sh[0], 0u}, xe1 = {mh[1], mh[1], sh[1], 0u}; PIN(xe0); PIN(xe1); NOPS(1);
    MF16A(a1[0][0], we[0], xe0); MF16A(a1[1][0], we[0], xe1);
    MF16A(a1[0][1], we[1], xe0); MF16A(a1[1][1], we[1], xe1);
    MF16A(a1[0][2], we[2], xe0); MF16A(a1[1][2], we[2], xe1);
    MF16A(a1[0][3], we[3], xe0); MF16A(a1[1][3], we[3], xe1);
  }
  NOPS(7);
  gelu8(a1[0][0], a1[0][1], gc[0], gd[0], gc[0], gd[0], go);
  st128(v4u{go[0], go[1], go[2], go[3]}, rw, l16 + 0, hp1); SCHED();
  gelu8(a1[1][0], a1[1][1], gc[1], gd[1], gc[1], gd[1], go);
  st128(v4u{go[0], go[1], go[2], go[3]}, rw, l16 + 0, hp1 + HT); SCHED();
  gelu8(a1[0][2], a1[0][3], gc[0], gd[0], gc[0], gd[0], go);
  st128(v4u{go[0], go[1], go[2], go[3]}, rw, l16 + 1024, hp1); SCHED();
  gelu8(a1[1][2], a1[1][3], gc[1], gd[1], gc[1], gd[1], go);
  st128(v4u{go[0], go[1], go[2], go[3]}, rw, l16 + 1024, hp1 + HT); SCHED();
  al[3] = trd(L + wa0 + 7328); SCHED();
  { const v4u a = {al[4].x, al[4].y, ah[4].x, ah[4].y}; MF16Z(a1[0][4], a, xf[0][0]); SCHED();
    ah[3] = trd(L + wa0 + 10912); SCHED();
    MF16Z(a1[1][4], a, xf[1][0]); } SCHED();
  al[4] = trd(L + wa0 + 7360); SCHED();
  { const v4u a = {al[0].x, al[0].y, ah[0].x, ah[0].y}; MF16Z(a1[0][5], a, xf[0][0]); SCHED();
    ah[4] = trd(L + wa0 + 10944); SCHED();
    MF16Z(a1[1][5], a, xf[1][0]); } SCHED();
  al[0] = trd(L + wa0 + 14464); SCHED();
  { const v4u a = {al[1].x, al[1].y, ah[1].x, ah[1].y}; MF16Z(a1[0][6], a, xf[0][0]); SCHED();
    ah[0] = trd(L + wa0 + 18048); SCHED();
    MF16Z(a1[1][6], a, xf[1][0]); } SCHED();
  al[1] = trd(L + wa0 + 14496); SCHED();
  { const v4u a = {al[2].x, al[2].y, ah[2].x, ah[2].y}; MF16(a1[0][4], a, xf[0][1]); SCHED();
    ah[1] = trd(L + wa0 + 18080); SCHED();
    MF16(a1[1][4], a, xf[1][1]); } SCHED();
  al[2] = trd(L + wa0 + 14528); SCHED();
  { const v4u a = {al[3].x, al[3].y, ah[3].x, ah[3].y}; MF16(a1[0][5], a, xf[0][1]); SCHED();
    ah[2] = trd(L + wa0 + 18112); SCHED();
    MF16(a1[1][5], a, xf[1][1]); } SCHED();
  al[3] = trd(L + wa0 + 21632); SCHED();
  { const v4u a = {al[4].x, al[4].y, ah[4].x, ah[4].y}; MF16(a1[0][6], a, xf[0][1]); SCHED();
    ah[3] = trd(L + wa0 + 25216); SCHED();
    MF16(a1[1][6], a, xf[1][1]); } SCHED();
  al[4] = trd(L + wa0 + 21664); SCHED();
  { const v4u a = {al[0].x, al[0].y, ah[0].x, ah[0].y}; MF16(a1[0][4], a, xf[0][2]); SCHED();
    ah[4] = trd(L + wa0 + 25248); SCHED();
    MF16(a1[1][4], a, xf[1][2]); } SCHED();
  al[0] = trd(L + wa0 + 21696); SCHED();
  { const v4u a = {al[1].x, al[1].y, ah[1].x, ah[1].y}; MF16(a1[0][5], a, xf[0][2]); SCHED();
    ah[0] = trd(L + wa0 + 25280); SCHED();
    MF16(a1[1][5], a, xf[1][2]); } SCHED();
  al[1] = trd(L + wa0 + 28800); SCHED();
  { const v4u a = {al[2].x, al[2].y, ah[2].x, ah[2].y}; MF16(a1[0][6], a, xf[0][2]); SCHED();
    ah[1] = trd(L + wa0 + 32384); SCHED();
    MF16(a1[1][6], a, xf[1][2]); } SCHED();
  al[2] = trd(L + wa0 + 28832); SCHED();
  { const v4u a = {al[3].x, al[3].y, ah[3].x, ah[3].y}; MF16(a1[0][4], a, xf[0][3]); SCHED();
    ah[2] = trd(L + wa0 + 32416); SCHED();
    MF16(a1[1][4], a, xf[1][3]); } SCHED();
  al[3] = trd(L + wa0 + 28864); SCHED();
  { const v4u a = {al[4].x, al[4].y, ah[4].x, ah[4].y}; MF16(a1[0][5], a, xf[0][3]); SCHED();
    ah[3] = trd(L + wa0 + 32448); SCHED();
    MF16(a1[1][5], a, xf[1][3]); } SCHED();
  al[4] = trd(L + wa0 + 35968); SCHED();
  { const v4u a = {al[0].x, al[0].y, ah[0].x, ah[0].y}; MF16(a1[0][6], a, xf[0][3]); SCHED();
    ah[4] = trd(L + wa0 + 39552); SCHED();
    MF16(a1[1][6], a, xf[1][3]); } SCHED();
  al[0] = trd(L + wa0 + 36000); SCHED();
  { const v4u a = {al[1].x, al[1].y, ah[1].x, ah[1].y}; MF16(a1[0][4], a, xf[0][4]); SCHED();
    ah[0] = trd(L + wa0 + 39584); SCHED();
    MF16(a1[1][4], a, xf[1][4]); } SCHED();
  al[1] = trd(L + wa0 + 36032); SCHED();
  { const v4u a = {al[2].x, al[2].y, ah[2].x, ah[2].y}; MF16(a1[0][5], a, xf[0][4]); SCHED();
    ah[1] = trd(L + wa0 + 39616); SCHED();
    MF16(a1[1][5], a, xf[1][4]); } SCHED();
  al[2] = trd(L + wa0 + 43136); SCHED();
  { const v4u a = {al[3].x, al[3].y, ah[3].x, ah[3].y}; MF16(a1[0][6], a, xf[0][4]); SCHED();
    ah[2] = trd(L + wa0 + 46720); SCHED();
    MF16(a1[1][6], a, xf[1][4]); } SCHED();
  al[3] = trd(L + wa0 + 43168); SCHED();
  { const v4u a = {al[4].x, al[4].y, ah[4].x, ah[4].y}; MF16(a1[0][4], a, xf[0][5]); SCHED();
    ah[3] = trd(L + wa0 + 46752); SCHED();
    MF16(a1[1][4], a, xf[1][5]); } SCHED();
  al[4] = trd(L + wa0 + 43200); SCHED();
  { const v4u a = {al[0].x, al[0].y, ah[0].x, ah[0].y}; MF16(a1[0][5], a, xf[0][5]); SCHED();
    ah[4] = trd(L + wa0 + 46784); SCHED();
    MF16(a1[1][5], a, xf[1][5]); } SCHED();
  al[0] = trd(L + wa0 + 50304); SCHED();
  { const v4u a = {al[1].x, al[1].y, ah[1].x, ah[1].y}; MF16(a1[0][6], a, xf[0][5]); SCHED();
    ah[0] = trd(L + wa0 + 53888); SCHED();
    MF16(a1[1][6], a, xf[1][5]); } SCHED();
  al[1] = trd(L + wa0 + 50336); SCHED();
  { const v4u a = {al[2].x, al[2].y, ah[2].x, ah[2].y}; MF16(a1[0][4], a, xf[0][6]); SCHED();
    ah[1] = trd(L + wa0 + 53920); SCHED();
    MF16(a1[1][4], a, xf[1][6]); } SCHED();
  al[2] = trd(L + wa0 + 50368); SCHED();
  { const v4u a = {al[3].x, al[3].y, ah[3].x, ah[3].y}; MF16(a1[0][5], a, xf[0][6]); SCHED();
    ah[2] = trd(L + wa0 + 53952); SCHED();
    MF16(a1[1][5], a, xf[1][6]); } SCHED();
  al[3] = trd(L + wa1 + 128); SCHED();
  { const v4u a = {al[4].x, al[4].y, ah[4].x, ah[4].y}; MF16(a1[0][6], a, xf[0][6]); SCHED();
    ah[3] = trd(L + wa1 + 3712); SCHED();
    MF16(a1[1][6], a, xf[1][6]); } SCHED();
  al[4] = trd(L + wa1 + 160); SCHED();
  { const v4u a = {al[0].x, al[0].y, ah[0].x, ah[0].y}; MF16(a1[0][4], a, xf[0][7]); SCHED();
    ah[4] = trd(L + wa1 + 3744); SCHED();
    MF16(a1[1][4], a, xf[1][7]); } SCHED();
  al[0] = trd(L + wa1 + 192); SCHED();
  { const v4u a = {al[1].x, al[1].y, ah[1].x, ah[1].y}; MF16(a1[0][5], a, xf[0][7]); SCHED();
    ah[0] = trd(L + wa1 + 3776); SCHED();
    MF16(a1[1][5], a, xf[1][7]); } SCHED();
  asm volatile("s_waitcnt vmcnt(0)" :  :: "memory");
  __builtin_amdgcn_s_barrier();
  if (w == 0) set_flag(rw, flm + 2 * FLAG_ARR, tag, lane);
  al[1] = trd(L + wa1 + 7296); SCHED();
  { const v4u a = {al[2].x, al[2].y, ah[2].x, ah[2].y}; MF16(a1[0][6], a, xf[0][7]); SCHED();
    ah[1] = trd(L + wa1 + 10880); SCHED();
    MF16(a1[1][6], a, xf[1][7]); } SCHED();
  al[2] = trd(L + wa1 + 7328); SCHED();
  { const v4u a = {al[3].x, al[3].y, ah[3].x, ah[3].y}; MF16(a1[0][4], a, xf[0][8]); SCHED();
    ah[2] = trd(L + wa1 + 10912); SCHED();
    MF16(a1[1][4], a, xf[1][8]); } SCHED();
  al[3] = trd(L + wa1 + 7360); SCHED();
  { const v4u a = {al[4].x, al[4].y, ah[4].x, ah[4].y}; MF16(a1[0][5], a, xf[0][8]); SCHED();
    ah[3] = trd(L + wa1 + 10944); SCHED();
    MF16(a1[1][5], a, xf[1][8]); } SCHED();
  al[4] = trd(L + wa1 + 14464); SCHED();
  { const v4u a = {al[0].x, al[0].y, ah[0].x, ah[0].y}; MF16(a1[0][6], a, xf[0][8]); SCHED();
    ah[4] = trd(L + wa1 + 18048); SCHED();
    MF16(a1[1][6], a, xf[1][8]); } SCHED();
  al[0] = trd(L + wa1 + 14496); SCHED();
  { const v4u a = {al[1].x, al[1].y, ah[1].x, ah[1].y}; MF16(a1[0][4], a, xf[0][9]); SCHED();
    ah[0] = trd(L + wa1 + 18080); SCHED();
    MF16(a1[1][4], a, xf[1][9]); } SCHED();
  al[1] = trd(L + wa1 + 14528); SCHED();
  { const v4u a = {al[2].x, al[2].y, ah[2].x, ah[2].y}; MF16(a1[0][5], a, xf[0][9]); SCHED();
    ah[1] = trd(L + wa1 + 18112); SCHED();
    MF16(a1[1][5], a, xf[1][9]); } SCHED();
  al[2] = trd(L + wa1 + 21632); SCHED();
  { const v4u a = {al[3].x, al[3].y, ah[3].x, ah[3].y}; MF16(a1[0][6], a, xf[0][9]); SCHED();
    ah[2] = trd(L + wa1 + 25216); SCHED();
    MF16(a1[1][6], a, xf[1][9]); } SCHED();
  al[3] = trd(L + wa1 + 21664); SCHED();
  { const v4u a = {al[4].x, al[4].y, ah[4].x, ah[4].y}; MF16(a1[0][4], a, xf[0][10]); SCHED();
    ah[3] = trd(L + wa1 + 25248); SCHED();
    MF16(a1[1][4], a, xf[1][10]); } SCHED();
  al[4] = trd(L + wa1 + 21696); SCHED();
  { const v4u a = {al[0].x, al[0].y, ah[0].x, ah[0].y}; MF16(a1[0][5], a, xf[0][10]); SCHED();
    ah[4] = trd(L + wa1 + 25280); SCHED();
    MF16(a1[1][5], a, xf[1][10]); } SCHED();
  al[0] = trd(L + wa1 + 28800); SCHED();
  { const v4u a = {al[1].x, al[1].y, ah[1].x, ah[1].y}; MF16(a1[0][6], a, xf[0][10]); SCHED();
    ah[0] = trd(L + wa1 + 32384); SCHED();
    MF16(a1[1][6], a, xf[1][10]); } SCHED();
  al[1] = trd(L + wa1 + 28832); SCHED();
  { const v4u a = {al[2].x, al[2].y, ah[2].x, ah[2].y}; MF16(a1[0][4], a, xf[0][11]); SCHED();
    ah[1] = trd(L + wa1 + 32416); SCHED();
    MF16(a1[1][4], a, xf[1][11]); } SCHED();
  al[2] = trd(L + wa1 + 28864); SCHED();
  { const v4u a = {al[3].x, al[3].y, ah[3].x, ah[3].y}; MF16(a1[0][5], a, xf[0][11]); SCHED();
    ah[2] = trd(L + wa1 + 32448); SCHED();
    MF16(a1[1][5], a, xf[1][11]); } SCHED();
  al[3] = trd(L + wa1 + 35968); SCHED();
  { const v4u a = {al[4].x, al[4].y, ah[4].x, ah[4].y}; MF16(a1[0][6], a, xf[0][11]); SCHED();
    ah[3] = trd(L + wa1 + 39552); SCHED();
    MF16(a1[1][6], a, xf[1][11]); } SCHED();
  al[4] = trd(L + wa1 + 36000); SCHED();
  { const v4u a = {al[0].x, al[0].y, ah[0].x, ah[0].y}; MF16(a1[0][4], a, xf[0][12]); SCHED();
    ah[4] = trd(L + wa1 + 39584); SCHED();
    MF16(a1[1][4], a, xf[1][12]); } SCHED();
  al[0] = trd(L + wa1 + 36032); SCHED();
  { const v4u a = {al[1].x, al[1].y, ah[1].x, ah[1].y}; MF16(a1[0][5], a, xf[0][12]); SCHED();
    ah[0] = trd(L + wa1 + 39616); SCHED();
    MF16(a1[1][5], a, xf[1][12]); } SCHED();
  al[1] = trd(L + wa1 + 43136); SCHED();
  { const v4u a = {al[2].x, al[2].y, ah[2].x, ah[2].y}; MF16(a1[0][6], a, xf[0][12]); SCHED();
    ah[1] = trd(L + wa1 + 46720); SCHED();
    MF16(a1[1][6], a, xf[1][12]); } SCHED();
  al[2] = trd(L + wa1 + 43168); SCHED();
  { const v4u a = {al[3].x, al[3].y, ah[3].x, ah[3].y}; MF16(a1[0][4], a, xf[0][13]); SCHED();
    ah[2] = trd(L + wa1 + 46752); SCHED();
    MF16(a1[1][4], a, xf[1][13]); } SCHED();
  al[3] = trd(L + wa1 + 43200); SCHED();
  { const v4u a = {al[4].x, al[4].y, ah[4].x, ah[4].y}; MF16(a1[0][5], a, xf[0][13]); SCHED();
    ah[3] = trd(L + wa1 + 46784); SCHED();
    MF16(a1[1][5], a, xf[1][13]); } SCHED();
  al[4] = trd(L + wa1 + 50304); SCHED();
  { const v4u a = {al[0].x, al[0].y, ah[0].x, ah[0].y}; MF16(a1[0][6], a, xf[0][13]); SCHED();
    ah[4] = trd(L + wa1 + 53888); SCHED();
    MF16(a1[1][6], a, xf[1][13]); } SCHED();
  al[0] = trd(L + wa1 + 50336); SCHED();
  { const v4u a = {al[1].x, al[1].y, ah[1].x, ah[1].y}; MF16(a1[0][4], a, xf[0][14]); SCHED();
    ah[0] = trd(L + wa1 + 53920); SCHED();
    MF16(a1[1][4], a, xf[1][14]); } SCHED();
  al[1] = trd(L + wa1 + 50368); SCHED();
  { const v4u a = {al[2].x, al[2].y, ah[2].x, ah[2].y}; MF16(a1[0][5], a, xf[0][14]); SCHED();
    ah[1] = trd(L + wa1 + 53952); SCHED();
    MF16(a1[1][5], a, xf[1][14]); } SCHED();
  { const v4u a = {al[3].x, al[3].y, ah[3].x, ah[3].y}; MF16(a1[0][6], a, xf[0][14]); SCHED();
    MF16(a1[1][6], a, xf[1][14]); } SCHED();
  { const v4u a = {al[4].x, al[4].y, ah[4].x, ah[4].y}; MF16(a1[0][4], a, xf[0][15]); SCHED();
    MF16(a1[1][4], a, xf[1][15]); } SCHED();
  { const v4u a = {al[0].x, al[0].y, ah[0].x, ah[0].y}; MF16(a1[0][5], a, xf[0][15]); SCHED();
    MF16(a1[1][5], a, xf[1][15]); } SCHED();
  { const v4u a = {al[1].x, al[1].y, ah[1].x, ah[1].y}; MF16(a1[0][6], a, xf[0][15]); SCHED();
    MF16(a1[1][6], a, xf[1][15]); } SCHED();
  { v4u xe0 = {mh[0], mh[0], sh[0], 0u}, xe1 = {mh[1], mh[1], sh[1], 0u}; PIN(xe0); PIN(xe1); NOPS(1);
    MF16A(a1[0][4], we[4], xe0); MF16A(a1[1][4], we[4], xe1);
    MF16A(a1[0][5], we[5], xe0); MF16A(a1[1][5], we[5], xe1);
    MF16A(a1[0][6], we[6], xe0); MF16A(a1[1][6], we[6], xe1);
  }
  NOPS(7);
  ldflag(fh, rw, fo, flw + 2 * FLAG_ARR);
  gelu8(a1[0][4], a1[0][5], gc[0], gd[0], gc[0], gd[0], go);
  st128(v4u{go[0], go[1], go[2], go[3]}, rw, l16 + 2048, hp1); SCHED();
  gelu8(a1[1][4], a1[1][5], gc[1], gd[1], gc[1], gd[1], go);
  st128(v4u{go[0], go[1], go[2], go[3]}, rw, l16 + 2048, hp1 + HT); SCHED();
  gelu8(a1[0][6], a1[1][6], gc[0], gd[0], gc[1], gd[1], go); swap_rows(go);
  st128(v4u{go[0], go[1], go[2], go[3]}, rw, h6o, hp61); SCHED();
  asm volatile("s_waitcnt vmcnt(3)" : "+v"(fh) :: "memory");
  if (__builtin_expect(!all_eq(fh, tag), 0)) poll(rw, fo, flw + 2 * FLAG_ARR, tag);
  ldc128<0>(hf[0][0], rw, l16, hq1[0] + 0);
  ldc128<0>(hf[0][1], rw, l16, hq1[1] + 0);
  ldc128<0>(hf[0][2], rw, l16, hq1[2] + 0);
  ldc128<0>(hf[0][3], rw, l16, hq1[3] + 0);
  ldc128<1024>(hf[1][0], rw, l16, hq1[0] + 0);
  ldc128<1024>(hf[1][1], rw, l16, hq1[1] + 0);
  ldc128<1024>(hf[1][2], rw, l16, hq1[2] + 0);
  ldc128<1024>(hf[1][3], rw, l16, hq1[3] + 0);
  ldc128<0>(hf[4][0], rw, l16, hq1[0] + 4096);
  ldc128<0>(hf[4][1], rw, l16, hq1[1] + 4096);
  ldc128<0>(hf[4][2], rw, l16, hq1[2] + 4096);
  ldc128<0>(hf[4][3], rw, l16, hq1[3] + 4096);
  SCHED();
  asm volatile("" : "+v"(b2p[0]), "+v"(b2p[1]));
  residual(a2[3], xr, b2p); NOPS(1);
  asm volatile("s_waitcnt vmcnt(12)" :  :: "memory");
  __builtin_amdgcn_s_barrier();
  if (w == 0) set_flag(rw, flm + 3 * FLAG_ARR, tag, lane);
  asm volatile("s_waitcnt vmcnt(11)" :  :: "memory");
  MF16A0(a2[0][0], w2f[0][0], hf[0][0]);
  ldc128<1024>(hf[5][0], rw, l16, hq1[0] + 4096);
  MF16A0(a2[0][1], w2f[0][1], hf[0][0]);
  MF16A0(a2[0][2], w2f[0][2], hf[0][0]);
  asm volatile("s_waitcnt vmcnt(11)" :  :: "memory");
  MF16A0(a2[0][3], w2f[0][3], hf[0][0]);
  SCHED();
  MF16A0(a2[1][0], w2f[0][0], hf[0][1]);
  ldc128<1024>(hf[5][1], rw, l16, hq1[1] + 4096);
  MF16A0(a2[1][1], w2f[0][1], hf[0][1]);
  MF16A0(a2[1][2], w2f[0][2], hf[0][1]);
  asm volatile("s_waitcnt vmcnt(11)" :  :: "memory");
  MF16A0(a2[1][3], w2f[0][3], hf[0][1]);
  SCHED();
  MF16A0(a2[2][0], w2f[0][0], hf[0][2]);
  ldc128<1024>(hf[5][2], rw, l16, hq1[2] + 4096);
  MF16A0(a2[2][1], w2f[0][1], hf[0][2]);
  MF16A0(a2[2][2], w2f[0][2], hf[0][2]);
  asm volatile("s_waitcnt vmcnt(11)" :  :: "memory");
  MF16A0(a2[2][3], w2f[0][3], hf[0][2]);
  SCHED();
  MF16A(a2[3][0], w2f[0][0], hf[0][3]);
  ldc128<1024>(hf[5][3], rw, l16, hq1[3] + 4096);
  MF16A(a2[3][1], w2f[0][1], hf[0][3]);
  MF16A(a2[3][2], w2f[0][2], hf[0][3]);
  asm volatile("s_waitcnt vmcnt(11)" :  :: "memory");
  MF16A(a2[3][3], w2f[0][3], hf[0][3]);
  SCHED();
  MF16A(a2[0][0], w2f[1][0], hf[1][0]);
  ldc128<3072>(hf[7][0], rw, l16, hq1[0] + 4096);
  MF16A(a2[0][1], w2f[1][1], hf[1][0]);
  MF16A(a2[0][2], w2f[1][2], hf[1][0]);
  asm volatile("s_waitcnt vmcnt(11)" :  :: "memory");
  MF16A(a2[0][3], w2f[1][3], hf[1][0]);
  SCHED();
  MF16A(a2[1][0], w2f[1][0], hf[1][1]);
  ldc128<3072>(hf[7][1], rw, l16, hq1[1] + 4096);
  MF16A(a2[1][1], w2f[1][1], hf[1][1]);
  MF16A(a2[1][2], w2f[1][2], hf[1][1]);
  asm volatile("s_waitcnt vmcnt(11)" :  :: "memory");
  MF16A(a2[1][3], w2f[1][3], hf[1][1]);
  SCHED();
  MF16A(a2[2][0], w2f[1][0], hf[1][2]);
  ldc128<3072>(hf[7][2], rw, l16, hq1[2] + 4096);
  MF16A(a2[2][1], w2f[1][1], hf[1][2]);
  MF16A(a2[2][2], w2f[1][2], hf[1][2]);
  asm volatile("s_waitcnt vmcnt(11)" :  :: "memory");
  MF16A(a2[2][3], w2f[1][3], hf[1][2]);
  SCHED();
  MF16A(a2[3][0], w2f[1][0], hf[1][3]);
  ldc128<3072>(hf[7][3], rw, l16, hq1[3] + 4096);
  MF16A(a2[3][1], w2f[1][1], hf[1][3]);
  MF16A(a2[3][2], w2f[1][2], hf[1][3]);
  asm volatile("s_waitcnt vmcnt(11)" :  :: "memory");
  MF16A(a2[3][3], w2f[1][3], hf[1][3]);
  SCHED();
  MF16A(a2[0][0], w2f[4][0], hf[4][0]);
  ldc128<0>(hf[8][0], rw, l16, hq1[0] + 8192);
  MF16A(a2[0][1], w2f[4][1], hf[4][0]);
  MF16A(a2[0][2], w2f[4][2], hf[4][0]);
  asm volatile("s_waitcnt vmcnt(11)" :  :: "memory");
  MF16A(a2[0][3], w2f[4][3], hf[4][0]);
  SCHED();
  MF16A(a2[1][0], w2f[4][0], hf[4][1]);
  ldc128<0>(hf[8][1], rw, l16, hq1[1] + 8192);
  MF16A(a2[1][1], w2f[4][1], hf[4][1]);
  MF16A(a2[1][2], w2f[4][2], hf[4][1]);
  asm volatile("s_waitcnt vmcnt(11)" :  :: "memory");
  MF16A(a2[1][3], w2f[4][3], hf[4][1]);
  SCHED();
  MF16A(a2[2][0], w2f[4][0], hf[4][2]);
  ldc128<0>(hf[8][2], rw, l16, hq1[2] + 8192);
  MF16A(a2[2][1], w2f[4][1], hf[4][2]);
  MF16A(a2[2][2], w2f[4][2], hf[4][2]);
  asm volatile("s_waitcnt vmcnt(11)" :  :: "memory");
  MF16A(a2[2][3], w2f[4][3], hf[4][2]);
  SCHED();
  MF16A(a2[3][0], w2f[4][0], hf[4][3]);
  ldc128<0>(hf[8][3], rw, l16, hq1[3] + 8192);
  MF16A(a2[3][1], w2f[4][1], hf[4][3]);
  MF16A(a2[3][2], w2f[4][2], hf[4][3]);
  asm volatile("s_waitcnt vmcnt(11)" :  :: "memory");
  MF16A(a2[3][3], w2f[4][3], hf[4][3]);
  SCHED();
  MF16A(a2[0][0], w2f[5][0], hf[5][0]);
  ldc128<3072>(hf[11][0], rw, l16, hq1[0] + 8192);
  MF16A(a2[0][1], w2f[5][1], hf[5][0]);
  MF16A(a2[0][2], w2f[5][2], hf[5][0]);
  asm volatile("s_waitcnt vmcnt(11)" :  :: "memory");
  MF16A(a2[0][3], w2f[5][3], hf[5][0]);
  SCHED();
  ldflag(fh, rw, fo, flw + 3 * FLAG_ARR);
  MF16A(a2[1][0], w2f[5][0], hf[5][1]);
  ldc128<3072>(hf[11][1], rw, l16, hq1[1] + 8192);
  MF16A(a2[1][1], w2f[5][1], hf[5][1]);
  MF16A(a2[1][2], w2f[5][2], hf[5][1]);
  asm volatile("s_waitcnt vmcnt(12)" :  :: "memory");
  MF16A(a2[1][3], w2f[5][3], hf[5][1]);
  SCHED();
  MF16A(a2[2][0], w2f[5][0], hf[5][2]);
  ldc128<3072>(hf[11][2], rw, l16, hq1[2] + 8192);
  MF16A(a2[2][1], w2f[5][1], hf[5][2]);
  MF16A(a2[2][2], w2f[5][2], hf[5][2]);
  asm volatile("s_waitcnt vmcnt(12)" :  :: "memory");
  MF16A(a2[2][3], w2f[5][3], hf[5][2]);
  SCHED();
  MF16A(a2[3][0], w2f[5][0], hf[5][3]);
  ldc128<3072>(hf[11][3], rw, l16, hq1[3] + 8192);
  MF16A(a2[3][1], w2f[5][1], hf[5][3]);
  MF16A(a2[3][2], w2f[5][2], hf[5][3]);
  asm volatile("s_waitcnt vmcnt(12)" :  :: "memory");
  MF16A(a2[3][3], w2f[5][3], hf[5][3]);
  SCHED();
  MF16A(a2[0][0], w2f[7][0], hf[7][0]);
  ldc128<0>(hf[12][0], rw, l16, hq1[0] + 12288);
  MF16A(a2[0][1], w2f[7][1], hf[7][0]);
  MF16A(a2[0][2], w2f[7][2], hf[7][0]);
  asm volatile("s_waitcnt vmcnt(12)" :  :: "memory");
  MF16A(a2[0][3], w2f[7][3], hf[7][0]);
  SCHED();
  MF16A(a2[1][0], w2f[7][0], hf[7][1]);
  ldc128<0>(hf[12][1], rw, l16, hq1[1] + 12288);
  MF16A(a2[1][1], w2f[7][1], hf[7][1]);
  MF16A(a2[1][2], w2f[7][2], hf[7][1]);
  asm volatile("s_waitcnt vmcnt(12)" :  :: "memory");
  MF16A(a2[1][3], w2f[7][3], hf[7][1]);
  SCHED();
  MF16A(a2[2][0], w2f[7][0], hf[7][2]);
  ldc128<0>(hf[12][2], rw, l16, hq1[2] + 12288);
  MF16A(a2[2][1], w2f[7][1], hf[7][2]);
  MF16A(a2[2][2], w2f[7][2], hf[7][2]);
  asm volatile("s_waitcnt vmcnt(12)" :  :: "memory");
  MF16A(a2[2][3], w2f[7][3], hf[7][2]);
  SCHED();
  MF16A(a2[3][0], w2f[7][0], hf[7][3]);
  ldc128<0>(hf[12][3], rw, l16, hq1[3] + 12288);
  MF16A(a2[3][1], w2f[7][1], hf[7][3]);
  MF16A(a2[3][2], w2f[7][2], hf[7][3]);
  asm volatile("s_waitcnt vmcnt(12)" :  :: "memory");
  MF16A(a2[3][3], w2f[7][3], hf[7][3]);
  SCHED();
  MF16A(a2[0][0], w2f[8][0], hf[8][0]);
  MF16A(a2[0][1], w2f[8][1], hf[8][0]);
  MF16A(a2[0][2], w2f[8][2], hf[8][0]);
  asm volatile("s_waitcnt vmcnt(11)" :  :: "memory");
  MF16A(a2[0][3], w2f[8][3], hf[8][0]);
  SCHED();
  MF16A(a2[1][0], w2f[8][0], hf[8][1]);
  MF16A(a2[1][1], w2f[8][1], hf[8][1]);
  MF16A(a2[1][2], w2f[8][2], hf[8][1]);
  asm volatile("s_waitcnt vmcnt(10)" :  :: "memory");
  MF16A(a2[1][3], w2f[8][3], hf[8][1]);
  SCHED();
  asm volatile("s_waitcnt vmcnt(7)" : "+v"(fh) :: "memory");
  if (__builtin_expect(!all_eq(fh, tag), 0)) poll(rw, fo, flw + 3 * FLAG_ARR, tag);
  ldc128<2048>(hf[2][0], rw, l16, hq1[0] + 0);
  ldc128<3072>(hf[3][0], rw, l16, hq1[0] + 0);
  SCHED();
  MF16A(a2[2][0], w2f[8][0], hf[8][2]);
  ldc128<2048>(hf[6][0], rw, l16, hq1[0] + 4096);
  MF16A(a2[2][1], w2f[8][1], hf[8][2]);
  MF16A(a2[2][2], w2f[8][2], hf[8][2]);
  MF16A(a2[2][3], w2f[8][3], hf[8][2]);
  SCHED();
  MF16A(a2[3][0], w2f[8][0], hf[8][3]);
  ldc128<1024>(hf[9][0], rw, l16, hq1[0] + 8192);
  MF16A(a2[3][1], w2f[8][1], hf[8][3]);
  MF16A(a2[3][2], w2f[8][2], hf[8][3]);
  MF16A(a2[3][3], w2f[8][3], hf[8][3]);
  SCHED();
  MF16A(a2[0][0], w2f[11][0], hf[11][0]);
  ldc128<2048>(hf[10][0], rw, l16, hq1[0] + 8192);
  MF16A(a2[0][1], w2f[11][1], hf[11][0]);
  MF16A(a2[0][2], w2f[11][2], hf[11][0]);
  asm volatile("s_waitcnt vmcnt(11)" :  :: "memory");
  MF16A(a2[0][3], w2f[11][3], hf[11][0]);
  SCHED();
  MF16A(a2[1][0], w2f[11][0], hf[11][1]);
  ldc128<1024>(hf[13][0], rw, l16, hq1[0] + 12288);
  MF16A(a2[1][1], w2f[11][1], hf[11][1]);
  MF16A(a2[1][2], w2f[11][2], hf[11][1]);
  asm volatile("s_waitcnt vmcnt(11)" :  :: "memory");
  MF16A(a2[1][3], w2f[11][3], hf[11][1]);
  SCHED();
  MF16A(a2[2][0], w2f[11][0], hf[11][2]);
  ldc128<2048>(hf[2][1], rw, l16, hq1[1] + 0);
  MF16A(a2[2][1], w2f[11][1], hf[11][2]);
  MF16A(a2[2][2], w2f[11][2], hf[11][2]);
  asm volatile("s_waitcnt vmcnt(11)" :  :: "memory");
  MF16A(a2[2][3], w2f[11][3], hf[11][2]);
  SCHED();
  MF16A(a2[3][0], w2f[11][0], hf[11][3]);
  ldc128<3072>(hf[3][1], rw, l16, hq1[1] + 0);
  MF16A(a2[3][1], w2f[11][1], hf[11][3]);
  MF16A(a2[3][2], w2f[11][2], hf[11][3]);
  asm volatile("s_waitcnt vmcnt(11)" :  :: "memory");
  MF16A(a2[3][3], w2f[11][3], hf[11][3]);
  SCHED();
  MF16A(a2[0][0], w2f[12][0], hf[12][0]);
  ldc128<2048>(hf[6][1], rw, l16, hq1[1] + 4096);
  MF16A(a2[0][1], w2f[12][1], hf[12][0]);
  MF16A(a2[0][2], w2f[12][2], hf[12][0]);
  asm volatile("s_waitcnt vmcnt(11)" :  :: "memory");
  MF16A(a2[0][3], w2f[12][3], hf[12][0]);
  SCHED();
  MF16A(a2[1][0], w2f[12][0], hf[12][1]);
  ldc128<1024>(hf[9][1], rw, l16, hq1[1] + 8192);
  MF16A(a2[1][1], w2f[12][1], hf[12][1]);
  MF16A(a2[1][2], w2f[12][2], hf[12][1]);
  asm volatile("s_waitcnt vmcnt(11)" :  :: "memory");
  MF16A(a2[1][3], w2f[12][3], hf[12][1]);
  SCHED();
  MF16A(a2[2][0], w2f[12][0], hf[12][2]);
  ldc128<2048>(hf[10][1], rw, l16, hq1[1] + 8192);
  MF16A(a2[2][1], w2f[12][1], hf[12][2]);
  MF16A(a2[2][2], w2f[12][2], hf[12][2]);
  asm volatile("s_waitcnt vmcnt(11)" :  :: "memory");
  MF16A(a2[2][3], w2f[12][3], hf[12][2]);
  SCHED();
  MF16A(a2[3][0], w2f[12][0], hf[12][3]);
  ldc128<1024>(hf[13][1], rw, l16, hq1[1] + 12288);
  MF16A(a2[3][1], w2f[12][1], hf[12][3]);
  MF16A(a2[3][2], w2f[12][2], hf[12][3]);
  asm volatile("s_waitcnt vmcnt(11)" :  :: "memory");
  MF16A(a2[3][3], w2f[12][3], hf[12][3]);
  SCHED();
  MF16A(a2[0][0], w2f[2][0], hf[2][0]);
  ldc128<2048>(hf[2][2], rw, l16, hq1[2] + 0);
  MF16A(a2[0][1], w2f[2][1], hf[2][0]);
  MF16A(a2[0][2], w2f[2][2], hf[2][0]);
  asm volatile("s_waitcnt vmcnt(11)" :  :: "memory");
  MF16A(a2[0][3], w2f[2][3], hf[2][0]);
  SCHED();
  MF16A(a2[0][0], w2f[3][0], hf[3][0]);
  ldc128<3072>(hf[3][2], rw, l16, hq1[2] + 0);
  MF16A(a2[0][1], w2f[3][1], hf[3][0]);
  MF16A(a2[0][2], w2f[3][2], hf[3][0]);
  asm volatile("s_waitcnt vmcnt(11)" :  :: "memory");
  MF16A(a2[0][3], w2f[3][3], hf[3][0]);
  SCHED();
  MF16A(a2[0][0], w2f[6][0], hf[6][0]);
  ldc128<2048>(hf[6][2], rw, l16, hq1[2] + 4096);
  MF16A(a2[0][1], w2f[6][1], hf[6][0]);
  MF16A(a2[0][2], w2f[6][2], hf[6][0]);
  asm volatile("s_waitcnt vmcnt(11)" :  :: "memory");
  MF16A(a2[0][3], w2f[6][3], hf[6][0]);
  SCHED();
  MF16A(a2[0][0], w2f[9][0], hf[9][0]);
  ldc128<1024>(hf[9][2], rw, l16, hq1[2] + 8192);
  MF16A(a2[0][1], w2f[9][1], hf[9][0]);
  MF16A(a2[0][2], w2f[9][2], hf[9][0]);
  asm volatile("s_waitcnt vmcnt(11)" :  :: "memory");
  MF16A(a2[0][3], w2f[9][3], hf[9][0]);
  SCHED();
  MF16A(a2[0][0], w2f[10][0], hf[10][0]);
  ldc128<2048>(hf[10][2], rw, l16, hq1[2] + 8192);
  MF16A(a2[0][1], w2f[10][1], hf[10][0]);
  MF16A(a2[0][2], w2f[10][2], hf[10][0]);
  asm volatile("s_waitcnt vmcnt(11)" :  :: "memory");
  MF16A(a2[0][3], w2f[10][3], hf[10][0]);
  SCHED();
  MF16A(a2[0][0], w2f[13][0], hf[13][0]);
  ldc128<1024>(hf[13][2], rw, l16, hq1[2] + 12288);
  MF16A(a2[0][1], w2f[13][1], hf[13][0]);
  MF16A(a2[0][2], w2f[13][2], hf[13][0]);
  asm volatile("s_waitcnt vmcnt(11)" :  :: "memory");
  MF16A(a2[0][3], w2f[13][3], hf[13][0]);
  SCHED();
  MF16A(a2[1][0], w2f[2][0], hf[2][1]);
  ldc128<2048>(hf[2][3], rw, l16, hq1[3] + 0);
  MF16A(a2[1][1], w2f[2][1], hf[2][1]);
  MF16A(a2[1][2], w2f[2][2], hf[2][1]);
  asm volatile("s_waitcnt vmcnt(11)" :  :: "memory");
  MF16A(a2[1][3], w2f[2][3], hf[2][1]);
  SCHED();
  MF16A(a2[1][0], w2f[3][0], hf[3][1]);
  ldc128<3072>(hf[3][3], rw, l16, hq1[3] + 0);
  MF16A(a2[1][1], w2f[3][1], hf[3][1]);
  SCHED(); *(LAS f32x4*)(L + ro[0] + 0) = a2[0][0]; SCHED();
  MF16A(a2[1][2], w2f[3][2], hf[3][1]);
  asm volatile("s_waitcnt vmcnt(11)" :  :: "memory");
  MF16A(a2[1][3], w2f[3][3], hf[3][1]);
  SCHED();
  MF16A(a2[1][0], w2f[6][0], hf[6][1]);
  ldc128<2048>(hf[6][3], rw, l16, hq1[3] + 4096);
  SCHED(); *(LAS f32x4*)(L + ro[0] + 1024) = a2[0][1]; SCHED();
  MF16A(a2[1][1], w2f[6][1], hf[6][1]);
  MF16A(a2[1][2], w2f[6][2], hf[6][1]);
  asm volatile("s_waitcnt vmcnt(11)" :  :: "memory");
  MF16A(a2[1][3], w2f[6][3], hf[6][1]);
  SCHED(); *(LAS f32x4*)(L + ro[0] + 2048) = a2[0][2]; SCHED();
  SCHED();
  MF16A(a2[1][0], w2f[9][0], hf[9][1]);
  ldc128<1024>(hf[9][3], rw, l16, hq1[3] + 8192);
  MF16A(a2[1][1], w2f[9][1], hf[9][1]);
  MF16A(a2[1][2], w2f[9][2], hf[9][1]);
  asm volatile("s_waitcnt vmcnt(11)" :  :: "memory");
  SCHED(); *(LAS f32x4*)(L + ro[0] + 3072) = a2[0][3]; SCHED();
  MF16A(a2[1][3], w2f[9][3], hf[9][1]);
  SCHED();
  MF16A(a2[1][0], w2f[10][0], hf[10][1]);
  ldc128<2048>(hf[10][3], rw, l16, hq1[3] + 8192);
  MF16A(a2[1][1], w2f[10][1], hf[10][1]);
  MF16A(a2[1][2], w2f[10][2], hf[10][1]);
  asm volatile("s_waitcnt vmcnt(11)" :  :: "memory");
  MF16A(a2[1][3], w2f[10][3], hf[10][1]);
  SCHED();
  MF16A(a2[1][0], w2f[13][0], hf[13][1]);
  ldc128<1024>(hf[13][3], rw, l16, hq1[3] + 12288);
  MF16A(a2[1][1], w2f[13][1], hf[13][1]);
  MF16A(a2[1][2], w2f[13][2], hf[13][1]);
  asm volatile("s_waitcnt vmcnt(11)" :  :: "memory");
  MF16A(a2[1][3], w2f[13][3], hf[13][1]);
  SCHED();
  MF16A(a2[2][0], w2f[2][0], hf[2][2]);
  MF16A(a2[2][1], w2f[2][1], hf[2][2]);
  MF16A(a2[2][2], w2f[2][2], hf[2][2]);
  asm volatile("s_waitcnt vmcnt(10)" :  :: "memory");
  MF16A(a2[2][3], w2f[2][3], hf[2][2]);
  SCHED();
  MF16A(a2[2][0], w2f[3][0], hf[3][2]);
  MF16A(a2[2][1], w2f[3][1], hf[3][2]);
  SCHED(); *(LAS f32x4*)(L + ro[1] + 0) = a2[1][0]; SCHED();
  MF16A(a2[2][2], w2f[3][2], hf[3][2]);
  asm volatile("s_waitcnt vmcnt(9)" :  :: "memory");
  MF16A(a2[2][3], w2f[3][3], hf[3][2]);
  SCHED();
  MF16A(a2[2][0], w2f[6][0], hf[6][2]);
  SCHED(); *(LAS f32x4*)(L + ro[1] + 1024) = a2[1][1]; SCHED();
  MF16A(a2[2][1], w2f[6][1], hf[6][2]);
  MF16A(a2[2][2], w2f[6][2], hf[6][2]);
  asm volatile("s_waitcnt vmcnt(8)" :  :: "memory");
  MF16A(a2[2][3], w2f[6][3], hf[6][2]);
  SCHED(); *(LAS f32x4*)(L + ro[1] + 2048) = a2[1][2]; SCHED();
  SCHED();
  MF16A(a2[2][0], w2f[9][0], hf[9][2]);
  MF16A(a2[2][1], w2f[9][1], hf[9][2]);
  MF16A(a2[2][2], w2f[9][2], hf[9][2]);
  asm volatile("s_waitcnt vmcnt(7)" :  :: "memory");
  SCHED(); *(LAS f32x4*)(L + ro[1] + 3072) = a2[1][3]; SCHED();
  MF16A(a2[2][3], w2f[9][3], hf[9][2]);
  SCHED();
  MF16A(a2[2][0], w2f[10][0], hf[10][2]);
  MF16A(a2[2][1], w2f[10][1], hf[10][2]);
  MF16A(a2[2][2], w2f[10][2], hf[10][2]);
  asm volatile("s_waitcnt vmcnt(6)" :  :: "memory");
  MF16A(a2[2][3], w2f[10][3], hf[10][2]);
  SCHED();
  MF16A(a2[2][0], w2f[13][0], hf[13][2]);
  MF16A(a2[2][1], w2f[13][1], hf[13][2]);
  MF16A(a2[2][2], w2f[13][2], hf[13][2]);
  asm volatile("s_waitcnt vmcnt(5)" :  :: "memory");
  MF16A(a2[2][3], w2f[13][3], hf[13][2]);
  SCHED();
  MF16A(a2[3][0], w2f[2][0], hf[2][3]);
  MF16A(a2[3][1], w2f[2][1], hf[2][3]);
  MF16A(a2[3][2], w2f[2][2], hf[2][3]);
  asm volatile("s_waitcnt vmcnt(4)" :  :: "memory");
  MF16A(a2[3][3], w2f[2][3], hf[2][3]);
  SCHED();
  MF16A(a2[3][0], w2f[3][0], hf[3][3]);
  MF16A(a2[3][1], w2f[3][1], hf[3][3]);
  SCHED(); *(LAS f32x4*)(L + ro[2] + 0) = a2[2][0]; SCHED();
  MF16A(a2[3][2], w2f[3][2], hf[3][3]);
  asm volatile("s_waitcnt vmcnt(3)" :  :: "memory");
  MF16A(a2[3][3], w2f[3][3], hf[3][3]);
  SCHED();
  MF16A(a2[3][0], w2f[6][0], hf[6][3]);
  SCHED(); *(LAS f32x4*)(L + ro[2] + 1024) = a2[2][1]; SCHED();
  MF16A(a2[3][1], w2f[6][1], hf[6][3]);
  MF16A(a2[3][2], w2f[6][2], hf[6][3]);
  asm volatile("s_waitcnt vmcnt(2)" :  :: "memory");
  MF16A(a2[3][3], w2f[6][3], hf[6][3]);
  SCHED(); *(LAS f32x4*)(L + ro[2] + 2048) = a2[2][2]; SCHED();
  SCHED();
  MF16A(a2[3][0], w2f[9][0], hf[9][3]);
  MF16A(a2[3][1], w2f[9][1], hf[9][3]);
  MF16A(a2[3][2], w2f[9][2], hf[9][3]);
  asm volatile("s_waitcnt vmcnt(1)" :  :: "memory");
  SCHED(); *(LAS f32x4*)(L + ro[2] + 3072) = a2[2][3]; SCHED();
  MF16A(a2[3][3], w2f[9][3], hf[9][3]);
  SCHED();
  MF16A(a2[3][0], w2f[10][0], hf[10][3]);
  BAR();
  SCHED(); rin[0] = *(LAS f32x4*)(L + ri + 0); SCHED();
  SCHED(); rin[1] = *(LAS f32x4*)(L + ri + 1024); SCHED();
  MF16A(a2[3][1], w2f[10][1], hf[10][3]);
  SCHED(); rin[2] = *(LAS f32x4*)(L + ri + 2048); SCHED();
  SCHED(); rin[3] = *(LAS f32x4*)(L + ri + 3072); SCHED();
  MF16A(a2[3][2], w2f[10][2], hf[10][3]);
  asm volatile("s_waitcnt vmcnt(0)" :  :: "memory");
  SCHED(); rin[4] = *(LAS f32x4*)(L + ri + 4096); SCHED();
  SCHED(); rin[5] = *(LAS f32x4*)(L + ri + 5120); SCHED();
  MF16A(a2[3][3], w2f[10][3], hf[10][3]);
  SCHED(); rin[6] = *(LAS f32x4*)(L + ri + 6144); SCHED();
  SCHED(); rin[7] = *(LAS f32x4*)(L + ri + 7168); SCHED();
  SCHED();
  MF16A(a2[3][0], w2f[13][0], hf[13][3]);
  SCHED(); rin[8] = *(LAS f32x4*)(L + ri + 8192); SCHED();
  SCHED(); rin[9] = *(LAS f32x4*)(L + ri + 9216); SCHED();
  MF16A(a2[3][1], w2f[13][1], hf[13][3]);
  SCHED(); rin[10] = *(LAS f32x4*)(L + ri + 10240); SCHED();
  SCHED(); rin[11] = *(LAS f32x4*)(L + ri + 11264); SCHED();
  MF16A(a2[3][2], w2f[13][2], hf[13][3]);
  MF16A(a2[3][3], w2f[13][3], hf[13][3]);
  SCHED();
  NOPS(7);
  finish16(rin, a2[3], xr);
  *(LAS v4u*)(L + oo) = xr[0]; *(LAS v4u*)(L + (oo ^ 16)) = xr[1];
  { const v4u o0 = *(LAS v4u*)(L + oi), o1 = *(LAS v4u*)(L + oi + 1024);
    st128(o0, rout, oso, (64 * tb + 8 * w) * 1024 + 128 * cb); st128(o1, rout, oso + 1024, (64 * tb + 8 * w) * 1024 + 128 * cb); }
  if (b == 0 && w == 0) set_flag(rw, WS_GEN, tag, lane);
  asm volatile(".Lh3_code_end:");
}

extern "C" void kernel_launch(void* const* d_in, const int* in_sizes, int n_in, void* d_out, int out_size, void* d_ws, size_t ws_size,
                              hipStream_t stream) {
  Params p;
  p.q = (const unsigned short*)d_in[0]; p.k = (const unsigned short*)d_in[1]; p.v = (const unsigned short*)d_in[2];
  p.sk = (const unsigned short*)d_in[3]; p.hk = (const unsigned short*)d_in[4]; p.hD = (const unsigned short*)d_in[5];
  p.w1 = (const unsigned short*)d_in[7]; p.b1 = (const unsigned short*)d_in[8]; p.w2 = (const unsigned short*)d_in[9];
  p.b2 = (const unsigned short*)d_in[10];
  p.out = (unsigned short*)d_out; p.ws = (unsigned char*)d_ws;
  (void)hipFuncSetAttribute((const void*)h3_mixer_kernel, hipFuncAttributeMaxDynamicSharedMemorySize, LDS_BYTES);
  hipLaunchKernelGGL(h3_mixer_kernel, dim3(256), dim3(256), LDS_BYTES, stream, p);
}
```
